```python
import torch

def _softmax(x, axis=-1):
    return torch.softmax(x, dim=axis)

def _gelu(x):
    return torch.nn.functional.gelu(x, approximate='tanh')

def _silu(x):
    return torch.nn.functional.silu(x)

def _sigmoid(x):
    return torch.sigmoid(x)

def _layernorm(x, axis=-1, eps=1e-05):
    m = x.mean(dim=axis, keepdim=True)
    v = x.var(dim=axis, keepdim=True, unbiased=False)
    return (x - m) / torch.sqrt(v + eps)

def _scatter_add(tgt, idx, src, axis=0):
    return tgt.clone().index_add_(axis, idx, src)

def _segment_sum(src, seg, n):
    out = torch.zeros((n,) + src.shape[1:], dtype=src.dtype)
    return out.index_add_(0, seg, src)

def _pad(x, pw):
    flat = tuple((v for pair in reversed(pw) for v in pair))
    return torch.nn.functional.pad(x, flat)
kernel_name = 'adaLN_cond__compressed_kv_cla__swiglu_ffn__decode'

def setup_inputs():

    _w = lambda a: (lambda t: t.to(torch.bfloat16) if t.is_floating_point() else t)(torch.as_tensor(a))

    def _A_setup():
        B, H, Sq, Sk, D, M = (2, 32, 1, 8192, 128, 640)
        Dc = 4 * D
        return {'x': _w((lambda *s: torch.randn(*s, dtype=torch.bfloat16) * 0.05)(B, Sq, M)), 'c_kv': _w((lambda *s: torch.randn(*s, dtype=torch.bfloat16) * 1.6)(B, Sk, Dc)), 'w_q': _w((lambda *s: torch.randn(*s, dtype=torch.bfloat16) * 1.6)(M, H, D) * M ** (-0.5)), 'w_uk': _w((lambda *s: torch.randn(*s, dtype=torch.bfloat16) * 1.6)(Dc, H, D) * Dc ** (-0.5)), 'w_uv': _w((lambda *s: torch.randn(*s, dtype=torch.bfloat16) * 1.6)(Dc, H, D) * Dc ** (-0.5)), 'w_o': _w((lambda *s: torch.randn(*s, dtype=torch.bfloat16) * 1.6)(H, D, M) * (H * D) ** (-0.5))}
    _A_in = _A_setup()

    def _B_setup():
        B, S, D, F = (2, 1, 640, 1728)
        return {'x': _w((lambda *s: torch.randn(*s, dtype=torch.bfloat16) * 0.05)(B, S, D)), 'w_gate': _w((lambda *s: torch.randn(*s, dtype=torch.bfloat16) * 0.05)(D, F)), 'w_up': _w((lambda *s: torch.randn(*s, dtype=torch.bfloat16) * 0.05)(D, F)), 'w_down': _w((lambda *s: torch.randn(*s, dtype=torch.bfloat16) * 0.05)(F, D))}
    _B_in = _B_setup()
    return {'A_x': _A_in['x'], 'A_c_kv': _A_in['c_kv'], 'A_w_q': _A_in['w_q'], 'A_w_uk': _A_in['w_uk'], 'A_w_uv': _A_in['w_uv'], 'A_w_o': _A_in['w_o'], 'B_x': _B_in['x'], 'B_w_gate': _B_in['w_gate'], 'B_w_up': _B_in['w_up'], 'B_w_down': _B_in['w_down'], 'c': _w((lambda *s: torch.randn(*s, dtype=torch.bfloat16) * 0.2)(2, 640) * 8.0), 'w_mod': _w((lambda *s: torch.randn(*s, dtype=torch.bfloat16) * 0.2)(640, 6 * 640) * 640 ** (-0.5) * 8.0)}

def reference(A_x, A_c_kv, A_w_q, A_w_uk, A_w_uv, A_w_o, B_x, B_w_gate, B_w_up, B_w_down, c, w_mod):

    def _A_ref(x, c_kv, w_q, w_uk, w_uv, w_o):
        B, Sq, M = (x.shape[0], x.shape[1], x.shape[2])
        Sk = c_kv.shape[1]
        H, D = (w_q.shape[1], w_q.shape[2])
        q = torch.einsum('bqm,mhd->bhqd', x, w_q)
        k = torch.einsum('bkc,chd->bhkd', c_kv, w_uk)
        v = torch.einsum('bkc,chd->bhkd', c_kv, w_uv)
        scores = torch.einsum('bhqd,bhkd->bhqk', q, k) * D ** (-0.5)
        off = Sk - Sq
        qi = torch.arange(Sq)[:, None]
        kj = torch.arange(Sk)[None, :]
        cmask = kj <= qi + off
        scores = torch.where(cmask[None, None], scores, -1000000000.0)
        p = _softmax(scores, axis=-1)
        ctx = torch.einsum('bhqk,bhkd->bhqd', p, v)
        return torch.einsum('bhqd,hdm->bqm', ctx, w_o)

    def _B_ref(x, w_gate, w_up, w_down):
        g = _silu(x @ w_gate)
        u = x @ w_up
        return g * u @ w_down
    x = A_x
    mod = (_silu(c) @ w_mod).reshape(c.shape[0], 6, 1, -1)
    sa, ca, ga, sb, cb, gb = (mod[:, 0].clone(), mod[:, 1].clone(), mod[:, 2].clone(), mod[:, 3].clone(), mod[:, 4].clone(), mod[:, 5].clone())
    h = _layernorm(x, axis=-1) * (1 + ca) + sa
    x = x + ga * _A_ref(h, A_c_kv, A_w_q, A_w_uk, A_w_uv, A_w_o)
    h = _layernorm(x, axis=-1) * (1 + cb) + sb
    x = x + gb * _B_ref(h, B_w_gate, B_w_up, B_w_down)
    return x
```

```cpp
#include <hip/hip_runtime.h>
#include <hip/hip_bf16.h>
#include <stdint.h>

typedef unsigned short u16;
__device__ __forceinline__ float bf2f(u16 b){ return __uint_as_float(((unsigned)b)<<16); }
__device__ __forceinline__ float silu(float x){ return x/(1.f+__expf(-x)); }

constexpr int B=2,H=32,SK=8192,D=128,M=640,DC=512,F=1728;

__global__ void gemv_kn(const float* __restrict__ in, const u16* __restrict__ W, float* __restrict__ out, int K, int N){
  __shared__ float red[4][2][128];
  int lane=threadIdx.x&63, w=threadIdx.x>>6;
  int n0=blockIdx.x*128+lane*2;
  float a0[2]={0,0}, a1[2]={0,0};
  if(n0<N){
  for(int k=w;k<K;k+=4){
    unsigned v=*(const unsigned*)(W+(size_t)k*N+n0);
    float w0=__uint_as_float(v<<16), w1=__uint_as_float(v&0xffff0000u);
    float x0=in[k], x1=in[K+k];
    a0[0]+=x0*w0; a0[1]+=x0*w1; a1[0]+=x1*w0; a1[1]+=x1*w1;
  }}
  red[w][0][lane*2]=a0[0]; red[w][0][lane*2+1]=a0[1]; red[w][1][lane*2]=a1[0]; red[w][1][lane*2+1]=a1[1];
  __syncthreads();
  int t=threadIdx.x; int b=t>>7, c=t&127;
  int n=blockIdx.x*128+c;
  if(n<N) out[b*N+n]=red[0][b][c]+red[1][b][c]+red[2][b][c]+red[3][b][c];
}
__global__ void k_silu_c(const u16* c, float* out){ int i=blockIdx.x*256+threadIdx.x; if(i<1280) out[i]=silu(bf2f(c[i])); }

__device__ float block_sum(float v, float* sh){
  for(int o=32;o>0;o>>=1) v+=__shfl_xor(v,o);
  int w=threadIdx.x>>6; if((threadIdx.x&63)==0) sh[w]=v; __syncthreads();
  float s=0; for(int i=0;i<10;i++) s+=sh[i]; __syncthreads(); return s;
}
__global__ void k_ln1(const u16* x, const float* mod, float* h){
  __shared__ float sh[16];
  int b=blockIdx.x, m=threadIdx.x;
  float v=bf2f(x[b*M+m]);
  float mean=block_sum(v,sh)/M; float d=v-mean; float var=block_sum(d*d,sh)/M;
  float ln=d*rsqrtf(var+1e-5f);
  h[b*M+m]=ln*(1.f+mod[b*3840+640+m])+mod[b*3840+m];
}
__global__ void k_qabs(const float* q, const u16* wuk, float* qabs){
  int gw=(blockIdx.x*256+threadIdx.x)>>6, lane=threadIdx.x&63;
  int c=gw>>5, h=gw&31;
  unsigned v=*(const unsigned*)(wuk+(size_t)c*4096+h*128+lane*2);
  float w0=__uint_as_float(v<<16), w1=__uint_as_float(v&0xffff0000u);
  float s0=q[h*128+lane*2]*w0+q[h*128+lane*2+1]*w1;
  float s1=q[4096+h*128+lane*2]*w0+q[4096+h*128+lane*2+1]*w1;
  for(int o=32;o>0;o>>=1){ s0+=__shfl_xor(s0,o); s1+=__shfl_xor(s1,o);}
  if(lane==0){ qabs[h*512+c]=s0*0.08838834764831845f; qabs[16384+h*512+c]=s1*0.08838834764831845f; }
}
__global__ void k_scores(const float* qabs, const u16* ckv, float* sc){
  int gw=(blockIdx.x*256+threadIdx.x)>>6, lane=threadIdx.x&63;
  int b=gw>>13, k=gw&8191;
  uint4 v=*(const uint4*)(ckv+((size_t)b*SK+k)*512+lane*8);
  float f[8]; unsigned u[4]={v.x,v.y,v.z,v.w};
  for(int i=0;i<4;i++){ f[2*i]=__uint_as_float(u[i]<<16); f[2*i+1]=__uint_as_float(u[i]&0xffff0000u);}
  for(int h=0;h<32;h++){
    const float* qp=qabs+b*16384+h*512+lane*8; float s=0;
    for(int i=0;i<8;i++) s+=qp[i]*f[i];
    for(int o=32;o>0;o>>=1) s+=__shfl_xor(s,o);
    if(lane==0) sc[((size_t)b*32+h)*SK+k]=s;
  }
}
__global__ void k_softmax(float* sc){
  __shared__ float sh[4];
  float* p=sc+(size_t)blockIdx.x*SK; int t=threadIdx.x;
  float m=-1e30f; for(int i=t;i<SK;i+=256) m=fmaxf(m,p[i]);
  for(int o=32;o>0;o>>=1) m=fmaxf(m,__shfl_xor(m,o));
  if((t&63)==0) sh[t>>6]=m; __syncthreads(); m=fmaxf(fmaxf(sh[0],sh[1]),fmaxf(sh[2],sh[3])); __syncthreads();
  float s=0; for(int i=t;i<SK;i+=256){ float e=__expf(p[i]-m); p[i]=e; s+=e;}
  for(int o=32;o>0;o>>=1) s+=__shfl_xor(s,o);
  if((t&63)==0) sh[t>>6]=s; __syncthreads(); s=sh[0]+sh[1]+sh[2]+sh[3];
  float r=1.f/s; for(int i=t;i<SK;i+=256) p[i]*=r;
}
__global__ void k_ctxc(const float* p, const u16* ckv, float* ctxc){
  int bh=blockIdx.x, b=bh>>5; int t=threadIdx.x; float a0=0,a1=0;
  const float* pp=p+(size_t)bh*SK;
  for(int k=0;k<SK;k++){
    unsigned v=*(const unsigned*)(ckv+((size_t)b*SK+k)*512+t*2);
    float w=pp[k]; a0+=w*__uint_as_float(v<<16); a1+=w*__uint_as_float(v&0xffff0000u);
  }
  ctxc[bh*512+t*2]=a0; ctxc[bh*512+t*2+1]=a1;
}
__global__ void k_ctx(const float* ctxc, const u16* wuv, float* ctx){
  int h=blockIdx.x, d=threadIdx.x; float a0=0,a1=0;
  for(int c=0;c<512;c++){ float w=bf2f(wuv[(size_t)c*4096+h*128+d]); a0+=w*ctxc[h*512+c]; a1+=w*ctxc[16384+h*512+c]; }
  ctx[h*128+d]=a0; ctx[4096+h*128+d]=a1;
}
__global__ void k_ln2(const u16* x, const float* mod, const float* attn, float* x1, float* h){
  __shared__ float sh[16];
  int b=blockIdx.x, m=threadIdx.x;
  float v=bf2f(x[b*M+m])+mod[b*3840+1280+m]*attn[b*M+m];
  x1[b*M+m]=v;
  float mean=block_sum(v,sh)/M; float d=v-mean; float var=block_sum(d*d,sh)/M;
  float ln=d*rsqrtf(var+1e-5f);
  h[b*M+m]=ln*(1.f+mod[b*3840+640*4+m])+mod[b*3840+640*3+m];
}
__global__ void k_act(const float* g, const float* u, float* a){ int i=blockIdx.x*256+threadIdx.x; if(i<2*F) a[i]=silu(g[i])*u[i]; }
__global__ void k_out(const float* x1, const float* mod, const float* y, __hip_bfloat16* out){
  int i=blockIdx.x*256+threadIdx.x; if(i<1280){ int b=i/640,m=i%640; out[i]=__float2bfloat16(x1[i]+mod[b*3840+3200+m]*y[i]); }
}

extern "C" void kernel_launch(void* const* d_in, const int* in_sizes, int n_in,
                              void* d_out, int out_size, void* d_ws, size_t ws_size,
                              hipStream_t stream) {
  const u16* x=(const u16*)d_in[0]; const u16* ckv=(const u16*)d_in[1]; const u16* wq=(const u16*)d_in[2];
  const u16* wuk=(const u16*)d_in[3]; const u16* wuv=(const u16*)d_in[4]; const u16* wo=(const u16*)d_in[5];
  const u16* wg=(const u16*)d_in[7]; const u16* wu=(const u16*)d_in[8]; const u16* wd=(const u16*)d_in[9];
  const u16* c=(const u16*)d_in[10]; const u16* wmod=(const u16*)d_in[11];
  float* ws=(float*)d_ws;
  float* sc_=ws; float* mod=sc_+1280; float* h=mod+7680; float* q=h+1280; float* qabs=q+8192; float* ctxc=qabs+32768;
  float* ctx=ctxc+32768; float* attn=ctx+8192; float* x1=attn+1280; float* h2=x1+1280; float* g=h2+1280; float* u=g+2*F; float* a=u+2*F; float* y=a+2*F; float* sc=y+1280+1024;
  k_silu_c<<<5,256,0,stream>>>(c,sc_);
  gemv_kn<<<30,256,0,stream>>>(sc_,wmod,mod,640,3840);
  k_ln1<<<2,640,0,stream>>>(x,mod,h);
  gemv_kn<<<32,256,0,stream>>>(h,wq,q,640,4096);
  k_qabs<<<512*32/4,256,0,stream>>>(q,wuk,qabs);
  k_scores<<<2*8192/4,256,0,stream>>>(qabs,ckv,sc);
  k_softmax<<<64,256,0,stream>>>(sc);
  k_ctxc<<<64,256,0,stream>>>(sc,ckv,ctxc);
  k_ctx<<<32,128,0,stream>>>(ctxc,wuv,ctx);
  gemv_kn<<<5,256,0,stream>>>(ctx,wo,attn,4096,640);
  k_ln2<<<2,640,0,stream>>>(x,mod,attn,x1,h2);
  gemv_kn<<<14,256,0,stream>>>(h2,wg,g,640,1728);
  gemv_kn<<<14,256,0,stream>>>(h2,wu,u,640,1728);
  k_act<<<14,256,0,stream>>>(g,u,a);
  gemv_kn<<<5,256,0,stream>>>(a,wd,y,1728,640);
  k_out<<<5,256,0,stream>>>(x1,mod,y,(__hip_bfloat16*)d_out);
}
```

```cpp
#include <hip/hip_runtime.h>
#include <hip/hip_bf16.h>
#include <stdint.h>

typedef unsigned short u16;
typedef unsigned int u32;
typedef unsigned long long u64;
typedef u32 v4u __attribute__((ext_vector_type(4)));
typedef u32 v2u __attribute__((ext_vector_type(2)));
typedef float f32x4 __attribute__((ext_vector_type(4)));
typedef float f32x2 __attribute__((ext_vector_type(2)));
typedef float f32x16 __attribute__((ext_vector_type(16)));
typedef short s16x4 __attribute__((ext_vector_type(4)));
typedef short s16x8 __attribute__((ext_vector_type(8)));
typedef __bf16 bf16x8 __attribute__((ext_vector_type(8)));
typedef __bf16 bf16x2 __attribute__((ext_vector_type(2)));
#define GAS __attribute__((address_space(1)))
#define LAS __attribute__((address_space(3)))
#define DI __device__ __forceinline__

constexpr u32 SENT = 0xFFFFFFFFu;
constexpr u32 SPIN_CAP = 1u << 17;

constexpr int WS_EPOCH = 0;
constexpr int WS_FLAGF = 256;
constexpr int WS_FLAGA = WS_FLAGF + 1024;
constexpr int WS_S1 = 4096;
constexpr int WS_S2 = WS_S1 + 5 * 4 * 640 * 4;
constexpr int WS_S3 = WS_S2 + 32 * 4 * 2 * 128 * 4;
constexpr int WS_S5 = WS_S3 + 2 * 32 * 512 * 2;
constexpr int WS_S6 = WS_S5 + 32 * 4 * 2 * 128 * 4;
constexpr int WS_MB = WS_S6 + 4 * 2 * 640 * 4;
constexpr int WS_S8 = WS_MB + 4 * 2 * 640 * 4;
constexpr int WS_ST = WS_S8 + 40 * 108 * 32 * 4;
constexpr int WS_S4 = WS_ST + 32 * 2 * 64 * 8;
constexpr int WS_END = WS_S4 + 32 * 4 * 2 * 64 * 256;
constexpr int WS_STAMP = WS_END;
static_assert(WS_S2 % 256 == 0 && WS_S3 % 256 == 0 && WS_S5 % 256 == 0 && WS_S6 % 256 == 0 && WS_MB % 256 == 0 && WS_S8 % 256 == 0 && WS_ST % 256 == 0 && WS_S4 % 256 == 0, "align");

constexpr int LDS_BYTES = 163840;

typedef __amdgpu_buffer_rsrc_t rsrc_t;
DI v4u wld128(rsrc_t r, int off) { return __builtin_amdgcn_raw_buffer_load_b128(r, off, 0, 16); }
DI v2u wld64(rsrc_t r, int off) { return __builtin_amdgcn_raw_buffer_load_b64(r, off, 0, 16); }
DI u32 wld32(rsrc_t r, int off) { return __builtin_amdgcn_raw_buffer_load_b32(r, off, 0, 16); }
DI void wst128(rsrc_t r, int off, v4u v) { __builtin_amdgcn_raw_buffer_store_b128(v, r, off, 0, 16); }
DI void wst64(rsrc_t r, int off, v2u v) { __builtin_amdgcn_raw_buffer_store_b64(v, r, off, 0, 16); }
DI void wst32(rsrc_t r, int off, u32 v) { __builtin_amdgcn_raw_buffer_store_b32(v, r, off, 0, 16); }
DI u32 canon(u32 x) { return x < 0xFFFFFFFEu ? x : 0xFFFFFFFEu; }
DI u32 canonf(float x) { return canon(__float_as_uint(x)); }
DI bool ok4(v4u v) { return v.x != SENT && v.y != SENT && v.z != SENT && v.w != SENT; }
DI bool ok2(v2u v) { return v.x != SENT && v.y != SENT; }
DI float bflo(u32 x) { return __uint_as_float(x << 16); }
DI float bfhi(u32 x) { return __uint_as_float(x & 0xffff0000u); }
DI u32 pk_bf16(float lo, float hi) { f32x2 f = {lo, hi}; bf16x2 h = __builtin_convertvector(f, bf16x2); return __builtin_bit_cast(u32, h); }
DI float silu_f(float x) { return x * __builtin_amdgcn_rcpf(1.f + __expf(-x)); }
DI v4u ldnt128(const void* p) { return __builtin_nontemporal_load((const GAS v4u*)p); }
DI f32x4 mfma16(v4u a, v4u b, f32x4 c) { return __builtin_amdgcn_mfma_f32_16x16x32_bf16(__builtin_bit_cast(bf16x8, a), __builtin_bit_cast(bf16x8, b), c, 0, 0, 0); }
DI f32x16 mfma32(v4u a, v4u b, f32x16 c) { return __builtin_amdgcn_mfma_f32_32x32x16_bf16(__builtin_bit_cast(bf16x8, a), __builtin_bit_cast(bf16x8, b), c, 0, 0, 0); }
#define BAR() asm volatile("s_waitcnt lgkmcnt(0)\n\ts_barrier" ::: "memory")
#define CBAR() asm volatile("" ::: "memory")
template <int N> DI void vmwait() { asm volatile("s_waitcnt vmcnt(%0)" :: "i"(N) : "memory"); }

template <int CTRL> DI float dpp(float x) { return __builtin_bit_cast(float, __builtin_amdgcn_mov_dpp(__builtin_bit_cast(int, x), CTRL, 0xf, 0xf, true)); }
DI float x16_sum(float x) { auto s = __builtin_amdgcn_permlane16_swap(__float_as_uint(x), __float_as_uint(x), false, false); return __uint_as_float(s[0]) + __uint_as_float(s[1]); }
DI float x32_sum(float x) { auto s = __builtin_amdgcn_permlane32_swap(__float_as_uint(x), __float_as_uint(x), false, false); return __uint_as_float(s[0]) + __uint_as_float(s[1]); }
DI float x16_max(float x) { auto s = __builtin_amdgcn_permlane16_swap(__float_as_uint(x), __float_as_uint(x), false, false); return fmaxf(__uint_as_float(s[0]), __uint_as_float(s[1])); }
DI float x32_max(float x) { auto s = __builtin_amdgcn_permlane32_swap(__float_as_uint(x), __float_as_uint(x), false, false); return fmaxf(__uint_as_float(s[0]), __uint_as_float(s[1])); }
DI float wave_sum(float x) { x += dpp<0xB1>(x); x += dpp<0x4E>(x); x += dpp<0x141>(x); x += dpp<0x140>(x); return x32_sum(x16_sum(x)); }
DI float wave_max(float x) { x = fmaxf(x, dpp<0xB1>(x)); x = fmaxf(x, dpp<0x4E>(x)); x = fmaxf(x, dpp<0x141>(x)); x = fmaxf(x, dpp<0x140>(x)); return x32_max(x16_max(x)); }

DI void dma_s(u32 ldsaddr, const void* sbase, u32 voff) {
  u32 keep;
  asm volatile("s_mov_b32 %0, m0\n\ts_mov_b32 m0, %1\n\ts_nop 0\n\tglobal_load_lds_dwordx4 %2, %3 nt\n\ts_mov_b32 m0, %0"
               : "=&s"(keep) : "s"(ldsaddr), "v"(voff), "s"(sbase) : "memory");
}
DI void dma_v(u32 ldsaddr, const void* vaddr) {
  u32 keep;
  asm volatile("s_mov_b32 %0, m0\n\ts_mov_b32 m0, %1\n\ts_nop 0\n\tglobal_load_lds_dwordx4 %2, off nt\n\ts_mov_b32 m0, %0"
               : "=&s"(keep) : "s"(ldsaddr), "v"(vaddr) : "memory");
}
DI u32 swz(u32 row) { return ((row & 3u) << 2) | ((row >> 2) & 3u); }
DI v4u ldsr128(const LAS char* p) { return *(const LAS v4u*)p; }
DI v4u tr2(const LAS char* p0, const LAS char* p1) {
  s16x4 a = __builtin_amdgcn_ds_read_tr16_b64_v4i16((LAS s16x4*)p0);
  s16x4 b = __builtin_amdgcn_ds_read_tr16_b64_v4i16((LAS s16x4*)p1);
  s16x8 v = __builtin_shufflevector(a, b, 0, 1, 2, 3, 4, 5, 6, 7);
  return __builtin_bit_cast(v4u, v);
}
DI void wait_flags(rsrc_t ws, int off, u32 E, int lane) {
  for (u32 sp = 0; sp < SPIN_CAP; ++sp) {
    CBAR();
    v4u f = wld128(ws, off + lane * 16);
    bool ok = f.x == E && f.y == ~E && f.z == E && f.w == ~E;
    if (__all(ok)) break;
    __builtin_amdgcn_s_sleep(1);
  }
}

#define STAMP(i) do { } while (0)

struct Ptrs { const u16 *x, *ckv, *wq, *wuk, *wuv, *wo, *wg, *wu, *wd, *c, *wmod; u16* out; char* ws; };

DI v4u frag64(const LAS char* img, int lane, int c, int ks) {
  const int g = lane >> 4, q = (lane & 15) >> 2, p = lane & 3;
  const LAS char* a = img + (32 * ks + 8 * g + q) * 64 + 32 * c + 8 * p;
  return tr2(a, a + 4 * 64);
}
DI v4u frag256(const LAS char* img, int lane, int c, int ks) {
  const int g = lane >> 4, q = (lane & 15) >> 2, p = lane & 3;
  const u32 r0 = 32 * ks + 8 * g + q, r1 = r0 + 4, ch = 2 * c + (p >> 1);
  return tr2(img + 256 * r0 + 16 * (ch ^ swz(r0)) + 8 * (p & 1), img + 256 * r1 + 16 * (ch ^ swz(r1)) + 8 * (p & 1));
}

constexpr int FL_WQ = 0, FL_WUV = 40960, FL_WO = FL_WUV + 32768, FL_SM = FL_WO + 65536;
constexpr int FL_RED1 = FL_SM, FL_HB = FL_RED1 + 2048, FL_QB = FL_HB + 1024, FL_CRED = FL_QB + 512, FL_CB = FL_CRED + 4096,
              FL_CTX = FL_CB + 512, FL_WRED = FL_CTX + 4096, FL_FIN = FL_WRED + 2048, FL_PF = FL_FIN + 1024;
static_assert(FL_PF + 1024 <= LDS_BYTES, "lds");

DI void f_role(const Ptrs& P, LAS char* L, rsrc_t ws, int p, int w, int lane
) {
  const int i16 = lane & 15, g = lane >> 4;
  const int h = p & 31, sl = p >> 5;
  u32 E = 0;
  u32 cv[4]; v4u wm[4];
  v4u xa, xb; v2u xs;
  const int cg = p % 20, ks1 = p / 20;
  if (w < 4) {
    if (w < 2) E = wld32(ws, WS_EPOCH);
    if (p < 100) {
      const int base = 128 * ks1 + 32 * w;
      const int bb = (lane >> 3) & 1, r1 = i16 >> 3, mat = (i16 >> 2) & 1, ch = i16 & 3;
#pragma unroll
      for (int t = 0; t < 4; ++t) cv[t] = *(const GAS u32*)(P.c + bb * 640 + base + 8 * t + 2 * g);
#pragma unroll
      for (int t = 0; t < 4; ++t) wm[t] = ldnt128(P.wmod + (size_t)(base + 8 * t + 2 * g + r1) * 3840 + mat * 640 + 32 * cg + 8 * ch);
    }
    if (w < 2) {
      xa = *(const GAS v4u*)(P.x + w * 640 + 8 * lane);
      xb = *(const GAS v4u*)(P.x + w * 640 + 512 + 8 * (lane & 15));
      xs = *(const GAS v2u*)(P.x + w * 640 + 160 * sl + 4 * (lane < 40 ? lane : 0));
    }
  } else if (w < 7) {
    const char* sb = (const char*)P.wq + (size_t)(160 * sl) * 8192 + h * 256;
    for (int n = w - 4; n < 40; n += 3) {
      const u32 row = 4 * n + g;
      dma_s(FL_WQ + n * 1024, sb, row * 8192 + ((i16 ^ swz(row)) << 4));
    }
  } else {
    E = wld32(ws, WS_EPOCH);
    const v4u S = {SENT, SENT, SENT, SENT};
    if (p < 100 && lane < 32) wst128(ws, WS_S1 + ((ks1 * 4 + (lane >> 3)) * 640 + 32 * cg) * 4 + (lane & 7) * 16, S);
    wst128(ws, WS_S2 + (h * 4 + sl) * 1024 + lane * 16, S);
    if (lane < 32) wst128(ws, WS_S3 + ((lane >> 4) * 32 + h) * 1024 + sl * 256 + i16 * 16, S);
    wst128(ws, WS_S5 + (h * 4 + sl) * 1024 + lane * 16, S);
    if (p < 80 && lane < 16) wst128(ws, WS_S6 + (((p / 20) * 2 + (lane >> 3)) * 640 + 32 * (p % 20)) * 4 + (lane & 7) * 16, S);
    vmwait<0>();
    if (lane == 0) { v2u f = {E + 1u, ~(E + 1u)}; wst64(ws, WS_FLAGF + p * 8, f); }
  }
  E += 1u;
  STAMP(1);
  if (w < 4 && p < 100) {
    const int jj = lane & 7;
    u32 msk[4];
#pragma unroll
    for (int r = 0; r < 4; ++r) msk[r] = (r == (jj >> 1)) ? (0xFFFFu << (16 * (jj & 1))) : 0u;
    f32x4 a0 = {0.f, 0.f, 0.f, 0.f}, a1 = {0.f, 0.f, 0.f, 0.f};
#pragma unroll
    for (int t = 0; t < 4; ++t) {
      const u32 sp = pk_bf16(silu_f(bflo(cv[t])), silu_f(bfhi(cv[t])));
      const u32 d0 = (sp & 0xFFFFu) * 0x10001u, d1 = (sp >> 16) * 0x10001u;
      const v4u B0 = {d0 & msk[0], d0 & msk[1], d0 & msk[2], d0 & msk[3]};
      const v4u B1 = {d1 & msk[0], d1 & msk[1], d1 & msk[2], d1 & msk[3]};
      a0 = mfma16(wm[t], B0, a0);
      a1 = mfma16(wm[t], B1, a1);
    }
    LAS float* red = (LAS float*)(L + FL_RED1) + w * 128;
    const int mat = (lane >> 4) & 1, bb = (lane >> 3) & 1;
#pragma unroll
    for (int r = 0; r < 4; ++r) {
      const float v = x32_sum(lane < 32 ? a0[r] : a1[r]);
      if (lane < 32) red[(mat * 2 + bb) * 32 + 8 * r + jj] = v;
    }
  }
  float mean = 0.f, rstd = 0.f;
  if (w < 2) {
    float v[16];
#pragma unroll
    for (int k = 0; k < 4; ++k) { v[2 * k] = bflo(xa[k]); v[2 * k + 1] = bfhi(xa[k]); v[8 + 2 * k] = bflo(xb[k]); v[9 + 2 * k] = bfhi(xb[k]); }
    const bool ex = lane < 16;
    float s = 0.f;
#pragma unroll
    for (int k = 0; k < 8; ++k) s += v[k] + (ex ? v[8 + k] : 0.f);
    mean = wave_sum(s) * (1.f / 640.f);
    float s2 = 0.f;
#pragma unroll
    for (int k = 0; k < 8; ++k) { const float d = v[k] - mean, e = v[8 + k] - mean; s2 += d * d + (ex ? e * e : 0.f); }
    rstd = rsqrtf(wave_sum(s2) * (1.f / 640.f) + 1e-5f);
  }
  STAMP(2);
  BAR();
  if (w == 2 && p < 100 && lane < 32) {
    const LAS f32x4* red = (const LAS f32x4*)(L + FL_RED1);
    const f32x4 s = (red[lane] + red[32 + lane]) + (red[64 + lane] + red[96 + lane]);
    const v4u o = {canonf(s[0]), canonf(s[1]), canonf(s[2]), canonf(s[3])};
    wst128(ws, WS_S1 + ((ks1 * 4 + (lane >> 3)) * 640 + 32 * cg) * 4 + (lane & 7) * 16, o);
  }
  v4u uk[4];
  if (w < 2) {
    wait_flags(ws, WS_FLAGF, E, lane);
    STAMP(3);
    const int m4 = lane < 40 ? lane : 0;
    f32x4 sa, ca;
    for (u32 sp = 0; sp < SPIN_CAP; ++sp) {
      CBAR();
      v4u t[10];
#pragma unroll
      for (int k = 0; k < 10; ++k) t[k] = wld128(ws, WS_S1 + (((k >> 1) * 4 + (k & 1) * 2 + w) * 640 + 160 * sl + 4 * m4) * 4);
      bool ok = true;
#pragma unroll
      for (int k = 0; k < 10; ++k) ok = ok && ok4(t[k]);
      sa = __builtin_bit_cast(f32x4, t[0]); ca = __builtin_bit_cast(f32x4, t[1]);
#pragma unroll
      for (int k = 1; k < 5; ++k) { sa += __builtin_bit_cast(f32x4, t[2 * k]); ca += __builtin_bit_cast(f32x4, t[2 * k + 1]); }
      if (__all(ok)) break;
    }
    const float x0 = bflo(xs.x), x1 = bfhi(xs.x), x2 = bflo(xs.y), x3 = bfhi(xs.y);
    const float h0 = (x0 - mean) * rstd * (1.f + ca[0]) + sa[0], h1 = (x1 - mean) * rstd * (1.f + ca[1]) + sa[1];
    const float h2 = (x2 - mean) * rstd * (1.f + ca[2]) + sa[2], h3 = (x3 - mean) * rstd * (1.f + ca[3]) + sa[3];
    if (lane < 40) { const v2u o = {pk_bf16(h0, h1), pk_bf16(h2, h3)}; *(LAS v2u*)(L + FL_HB + w * 320 + lane * 8) = o; }
    STAMP(4);
  } else if (w >= 4 && w < 7) {
    vmwait<0>();
  }
#pragma unroll
  for (int k = 0; k < 4; ++k) uk[k] = ldnt128(P.wuk + (size_t)(128 * sl + 16 * w + i16) * 4096 + h * 128 + 32 * k + 8 * g);
  BAR();
  {
    f32x4 acc = {0.f, 0.f, 0.f, 0.f};
#pragma unroll
    for (int k = 0; k < 5; ++k) {
      const v4u a = frag256(L + FL_WQ, lane, w, k);
      const v4u b = ldsr128(L + FL_HB + (lane & 1) * 320 + (32 * k + 8 * g) * 2);
      acc = mfma16(a, b, acc);
    }
    if (i16 < 2) { const v4u o = {canonf(acc[0]), canonf(acc[1]), canonf(acc[2]), canonf(acc[3])};
      wst128(ws, WS_S2 + ((h * 4 + sl) * 2 + i16) * 512 + (16 * w + 4 * g) * 4, o); }
  }
  STAMP(5);
  if (w == 0) {
    f32x4 q;
    for (u32 sp = 0; sp < SPIN_CAP; ++sp) {
      CBAR();
      v4u t[4];
#pragma unroll
      for (int k = 0; k < 4; ++k) t[k] = wld128(ws, WS_S2 + (h * 4 + k) * 1024 + lane * 16);
      bool ok = ok4(t[0]) && ok4(t[1]) && ok4(t[2]) && ok4(t[3]);
      q = (__builtin_bit_cast(f32x4, t[0]) + __builtin_bit_cast(f32x4, t[1])) + (__builtin_bit_cast(f32x4, t[2]) + __builtin_bit_cast(f32x4, t[3]));
      if (__all(ok)) break;
    }
    const v2u o = {pk_bf16(q[0], q[1]), pk_bf16(q[2], q[3])};
    *(LAS v2u*)(L + FL_QB + lane * 8) = o;
    STAMP(6);
  }
  BAR();
  {
    f32x4 acc = {0.f, 0.f, 0.f, 0.f};
#pragma unroll
    for (int k = 0; k < 4; ++k) acc = mfma16(uk[k], ldsr128(L + FL_QB + (lane & 1) * 256 + (32 * k + 8 * g) * 2), acc);
    constexpr float SC = 0.08838834764831845f * 1.4426950408889634f;
    if (i16 < 2) { const v2u o = {canon(pk_bf16(acc[0] * SC, acc[1] * SC)), canon(pk_bf16(acc[2] * SC, acc[3] * SC))};
      wst64(ws, WS_S3 + (i16 * 32 + h) * 1024 + (128 * sl + 16 * w + 4 * g) * 2, o); }
  }
  STAMP(7);
  const int kq = p / 20, ms = p % 20;
  if (w >= 4) {
    const char* sb = (const char*)P.wuv + (size_t)(128 * sl) * 8192 + h * 256;
    for (int n = w - 4; n < 32; n += 4) {
      const u32 row = 4 * n + g;
      dma_s(FL_WUV + n * 1024, sb, row * 8192 + ((i16 ^ swz(row)) << 4));
    }
    if (p < 80) {
      const char* so = (const char*)P.wo + (size_t)(1024 * kq) * 1280 + ms * 64;
      const u32 vo = (lane >> 2) * 1280 + (lane & 3) * 16;
      for (int n = w - 4; n < 64; n += 4) dma_s(FL_WO + n * 1024, so + (size_t)n * 16 * 1280, vo);
      vmwait<16>();
    } else {
      vmwait<0>();
    }
  } else if (w == 3) {
    E = wld32(ws, WS_EPOCH) + 1u;
    wait_flags(ws, WS_FLAGA, E, lane);
  }
  BAR();
  STAMP(8);
  {
    v4u uv[4];
#pragma unroll
    for (int k = 0; k < 4; ++k) uv[k] = frag256(L + FL_WUV, lane, w, k);
    const int b = w >> 2, k0 = 16 * (w & 3);
    v4u t[4]; v2u s;
    for (u32 sp = 0; sp < SPIN_CAP; ++sp) {
      CBAR();
      s = wld64(ws, WS_ST + ((h * 2 + b) * 64 + lane) * 8);
#pragma unroll
      for (int j = 0; j < 4; ++j) t[j] = wld128(ws, WS_S4 + ((((h * 4 + sl) * 2 + b) * 64 + k0 + 4 * j) * 256) + lane * 16);
      const bool ok = ok2(s) && ok4(t[0]) && ok4(t[1]) && ok4(t[2]) && ok4(t[3]);
      if (__all(ok)) break;
    }
    STAMP(9);
    const float m = __uint_as_float(s.x), l = __uint_as_float(s.y);
    const float M = wave_max(m);
    const float e = __builtin_amdgcn_exp2f(m - M);
    const float wt = e * __builtin_amdgcn_rcpf(wave_sum(e * l));
    float acc[8];
#pragma unroll
    for (int k = 0; k < 8; ++k) acc[k] = 0.f;
#pragma unroll
    for (int j = 0; j < 4; ++j) {
      const float wj = __shfl(wt, k0 + 4 * j + g);
#pragma unroll
      for (int k = 0; k < 4; ++k) { acc[2 * k] += wj * bflo(t[j][k]); acc[2 * k + 1] += wj * bfhi(t[j][k]); }
    }
#pragma unroll
    for (int k = 0; k < 8; ++k) acc[k] = x32_sum(x16_sum(acc[k]));
    if (lane < 16) {
      LAS f32x4* cr = (LAS f32x4*)(L + FL_CRED + w * 512 + lane * 32);
      cr[0] = f32x4{acc[0], acc[1], acc[2], acc[3]}; cr[1] = f32x4{acc[4], acc[5], acc[6], acc[7]};
    }
    BAR();
    if (w == 0) {
      const LAS f32x4* cr = (const LAS f32x4*)(L + FL_CRED + (lane >> 5) * 2048 + (lane & 31) * 16);
      const f32x4 v = (cr[0] + cr[32]) + (cr[64] + cr[96]);
      const v2u o = {pk_bf16(v[0], v[1]), pk_bf16(v[2], v[3])};
      *(LAS v2u*)(L + FL_CB + lane * 8) = o;
    }
    BAR();
    f32x4 a2 = {0.f, 0.f, 0.f, 0.f};
#pragma unroll
    for (int k = 0; k < 4; ++k) a2 = mfma16(uv[k], ldsr128(L + FL_CB + (lane & 1) * 256 + (32 * k + 8 * g) * 2), a2);
    if (i16 < 2) { const v4u o = {canonf(a2[0]), canonf(a2[1]), canonf(a2[2]), canonf(a2[3])};
      wst128(ws, WS_S5 + ((h * 4 + sl) * 2 + i16) * 512 + (16 * w + 4 * g) * 4, o); }
  }
  STAMP(10);
  if (p < 80) {
    v4u ao[8];
#pragma unroll
    for (int k = 0; k < 4; ++k) { ao[2 * k] = frag64(L + FL_WO, lane, 0, 4 * w + k); ao[2 * k + 1] = frag64(L + FL_WO, lane, 1, 4 * w + k); }
    f32x4 cx;
    for (u32 sp = 0; sp < SPIN_CAP; ++sp) {
      CBAR();
      v4u t[4];
#pragma unroll
      for (int k = 0; k < 4; ++k) t[k] = wld128(ws, WS_S5 + ((8 * kq + w) * 4 + k) * 1024 + lane * 16);
      const bool ok = ok4(t[0]) && ok4(t[1]) && ok4(t[2]) && ok4(t[3]);
      cx = (__builtin_bit_cast(f32x4, t[0]) + __builtin_bit_cast(f32x4, t[1])) + (__builtin_bit_cast(f32x4, t[2]) + __builtin_bit_cast(f32x4, t[3]));
      if (__all(ok)) break;
    }
    STAMP(11);
    const v2u o = {pk_bf16(cx[0], cx[1]), pk_bf16(cx[2], cx[3])};
    *(LAS v2u*)(L + FL_CTX + w * 512 + lane * 8) = o;
    asm volatile("s_waitcnt lgkmcnt(0)" ::: "memory");
    f32x4 a0 = {0.f, 0.f, 0.f, 0.f}, a1 = {0.f, 0.f, 0.f, 0.f};
#pragma unroll
    for (int k = 0; k < 4; ++k) {
      const v4u b = ldsr128(L + FL_CTX + w * 512 + (lane & 1) * 256 + (32 * k + 8 * g) * 2);
      a0 = mfma16(ao[2 * k], b, a0); a1 = mfma16(ao[2 * k + 1], b, a1);
    }
    if (i16 < 2) {
      LAS f32x4* wr = (LAS f32x4*)(L + FL_WRED + w * 256 + i16 * 128 + g * 16);
      wr[0] = a0; wr[4] = a1;
    }
    BAR();
    if (w == 0 && lane < 16) {
      const LAS f32x4* wr = (const LAS f32x4*)(L + FL_WRED + lane * 16);
      f32x4 v = wr[0];
#pragma unroll
      for (int k = 1; k < 8; ++k) v += wr[16 * k];
      const v4u oo = {canonf(v[0]), canonf(v[1]), canonf(v[2]), canonf(v[3])};
      wst128(ws, WS_S6 + ((kq * 2 + (lane >> 3)) * 640 + 32 * ms) * 4 + (lane & 7) * 16, oo);
    }
    STAMP(12);
  } else if (p >= 88) {
    const int fs = p - 88, bm = lane & 7, bb = bm >> 2, m0 = 16 * fs + 4 * (bm & 3);
    f32x4 x1, gb;
    if (w == 0) {
      const v2u xv = *(const GAS v2u*)(P.x + bb * 640 + m0);
      for (u32 sp = 0; sp < SPIN_CAP; ++sp) {
        CBAR();
        v4u t[6];
#pragma unroll
        for (int k = 0; k < 4; ++k) t[k] = wld128(ws, WS_S6 + ((k * 2 + bb) * 640 + m0) * 4);
        t[4] = wld128(ws, WS_MB + ((0 * 2 + bb) * 640 + m0) * 4);
        t[5] = wld128(ws, WS_MB + ((3 * 2 + bb) * 640 + m0) * 4);
        bool ok = true;
#pragma unroll
        for (int k = 0; k < 6; ++k) ok = ok && ok4(t[k]);
        const f32x4 at = (__builtin_bit_cast(f32x4, t[0]) + __builtin_bit_cast(f32x4, t[1])) + (__builtin_bit_cast(f32x4, t[2]) + __builtin_bit_cast(f32x4, t[3]));
        const f32x4 xf = {bflo(xv.x), bfhi(xv.x), bflo(xv.y), bfhi(xv.y)};
        x1 = xf + __builtin_bit_cast(f32x4, t[4]) * at;
        gb = __builtin_bit_cast(f32x4, t[5]);
        if (__all(ok)) break;
        __builtin_amdgcn_s_sleep(2);
      }
    }
    STAMP(11);
    f32x4 y;
    const int tid = w * 64 + lane;
    for (u32 sp = 0; sp < SPIN_CAP; ++sp) {
      CBAR();
      const v4u t0 = wld128(ws, WS_S8 + fs * (108 * 128) + tid * 16);
      v4u t1 = {0u, 0u, 0u, 0u};
      if (tid < 352) t1 = wld128(ws, WS_S8 + fs * (108 * 128) + (512 + tid) * 16);
      y = __builtin_bit_cast(f32x4, t0) + __builtin_bit_cast(f32x4, t1);
      if (__all(ok4(t0) && ok4(t1))) break;
    }
    STAMP(12);
#pragma unroll
    for (int k = 0; k < 4; ++k) { float v = y[k]; v += dpp<0x128>(v); y[k] = x32_sum(x16_sum(v)); }
    if (lane < 8) *(LAS f32x4*)(L + FL_FIN + w * 128 + lane * 16) = y;
    BAR();
    if (w == 0 && lane < 8) {
      const LAS f32x4* fr = (const LAS f32x4*)(L + FL_FIN + lane * 16);
      f32x4 v = fr[0];
#pragma unroll
      for (int k = 1; k < 8; ++k) v += fr[8 * k];
      const f32x4 o = x1 + gb * v;
      const v2u ob = {pk_bf16(o[0], o[1]), pk_bf16(o[2], o[3])};
      *(GAS v2u*)(P.out + bb * 640 + m0) = ob;
      if (p == 88 && lane == 0) wst32(ws, WS_EPOCH, E);
    }
    STAMP(13);
  }
}

constexpr int AL_CKV = 0, AL_Q = 131072;
constexpr int AL_P = AL_Q, AL_MAX = AL_Q + 8192, AL_SUM = AL_MAX + 512;
constexpr int AL_O = 0, AL_MW = 32768, AL_GU = AL_MW + 40960, AL_DN = AL_GU + 40960, AL_SM = AL_DN + 20480;
constexpr int AL_SB = AL_SM, AL_H2 = AL_SB + 2560, AL_STAT = AL_H2 + 2560, AL_RED = AL_STAT + 64, AL_ACT = AL_RED + 2048;
static_assert(AL_ACT + 128 <= LDS_BYTES, "lds");

DI void gemv640(LAS char* L, int img, int xb, int w, int lane) {
  const int i16 = lane & 15, g = lane >> 4;
  f32x4 a0 = {0.f, 0.f, 0.f, 0.f}, a1 = {0.f, 0.f, 0.f, 0.f};
  for (int ks = w; ks < 20; ks += 8) {
    const v4u f0 = frag64(L + img, lane, 0, ks), f1 = frag64(L + img, lane, 1, ks);
    const v4u b = ldsr128(L + xb + (lane & 1) * 1280 + (32 * ks + 8 * g) * 2);
    a0 = mfma16(f0, b, a0); a1 = mfma16(f1, b, a1);
  }
  if (i16 < 2) { LAS f32x4* wr = (LAS f32x4*)(L + AL_RED + w * 256 + i16 * 128 + g * 16); wr[0] = a0; wr[4] = a1; }
}

DI void a_role(const Ptrs& P, LAS char* L, rsrc_t ws, int j, int w, int lane
) {
  const int i16 = lane & 15, g = lane >> 4, l32 = lane & 31, hh = lane >> 5;
  const int b = j >> 6, kc = j & 63;
  u32 E = 0;
  f32x16 S;
  const int tid = w * 64 + lane;
  v4u sv = {0u, 0u, 0u, 0u};
  if (w >= 4) {
    const int lw = w - 4;
    const char* sb = (const char*)P.ckv + ((size_t)(b * 8192 + kc * 128 + 32 * lw)) * 1024;
    u32 vo[4];
#pragma unroll
    for (int i = 0; i < 4; ++i) vo[i] = g * 1024 + ((i16 ^ ((g << 2) | i)) << 4);
    for (int cq = 0; cq < 4; ++cq) {
#pragma unroll
      for (int i = 0; i < 8; ++i) dma_s(AL_CKV + cq * 32768 + (8 * lw + i) * 1024, sb + i * 4096 + cq * 256, vo[i & 3]);
    }
    STAMP(1);
    vmwait<24>();
  } else {
    E = wld32(ws, WS_EPOCH);
    if (j >= 48) {
      const v4u cvv = *(const GAS v4u*)(P.c + (tid < 160 ? tid : 0) * 8);
#pragma unroll
      for (int k = 0; k < 4; ++k) sv[k] = pk_bf16(silu_f(bflo(cvv[k])), silu_f(bfhi(cvv[k])));
    }
    const v4u Sv = {SENT, SENT, SENT, SENT};
#pragma unroll
    for (int k = 0; k < 8; ++k) { const int hd = 8 * w + k;
      wst128(ws, WS_S4 + ((((hd * 4 + g) * 2 + b) * 64 + kc) * 256) + i16 * 16, Sv); }
    if (w == 0 && lane < 32) { const v2u s2 = {SENT, SENT}; wst64(ws, WS_ST + ((lane * 2 + b) * 64 + kc) * 8, s2); }
    if (j < 108) {
      wst128(ws, WS_S8 + (10 * w + (lane >> 3)) * (108 * 128) + j * 128 + (lane & 7) * 16, Sv);
      if (lane < 16) wst128(ws, WS_S8 + (10 * w + 8 + (lane >> 3)) * (108 * 128) + j * 128 + (lane & 7) * 16, Sv);
    }
    if (j >= 48 && w == 1 && lane < 16) { const int col = 32 * (j - 48);
      wst128(ws, WS_MB + (((col / 640) * 2 + (lane >> 3)) * 640 + col % 640) * 4 + (lane & 7) * 16, Sv); }
    E += 1u;
    wait_flags(ws, WS_FLAGF, E, lane);
    STAMP(1);
    for (u32 sp = 0; sp < SPIN_CAP; ++sp) {
      CBAR();
      v4u t[8];
#pragma unroll
      for (int k = 0; k < 8; ++k) t[k] = wld128(ws, WS_S3 + (b * 32 + 8 * w + k) * 1024 + lane * 16);
      bool ok = true;
#pragma unroll
      for (int k = 0; k < 8; ++k) ok = ok && ok4(t[k]);
      if (__all(ok)) {
#pragma unroll
        for (int k = 0; k < 8; ++k) { const int hd = 8 * w + k; *(LAS v4u*)(L + AL_Q + hd * 1024 + ((lane ^ (hd & 15)) << 4)) = t[k]; }
        break;
      }
      __builtin_amdgcn_s_sleep(1);
    }
    vmwait<0>();
    STAMP(2);
  }
  BAR();
  if (w < 4) {
    if (w == 0 && lane == 0) { v2u f = {E, ~E}; wst64(ws, WS_FLAGA + j * 8, f); }
    v4u qf[32];
#pragma unroll
    for (int k = 0; k < 32; ++k) qf[k] = ldsr128(L + AL_Q + l32 * 1024 + (((2 * k + hh) ^ (l32 & 15)) << 4));
#pragma unroll
    for (int k = 0; k < 16; ++k) S[k] = 0.f;
    const u32 row = 32 * w + l32, sw = swz(row);
#pragma unroll
    for (int cq = 0; cq < 4; ++cq) {
      if (cq > 0) BAR();
#pragma unroll
      for (int s = 0; s < 8; ++s) {
        const v4u a = ldsr128(L + AL_CKV + cq * 32768 + row * 256 + (((2 * s + hh) ^ sw) << 4));
        S = mfma32(a, qf[8 * cq + s], S);
      }
    }
    STAMP(3);
    float m = S[0];
#pragma unroll
    for (int k = 1; k < 16; ++k) m = fmaxf(m, S[k]);
    m = x32_max(m);
    if (lane < 32) *(LAS float*)(L + AL_MAX + (w * 32 + lane) * 4) = m;
  } else {
    vmwait<16>(); BAR();
    vmwait<8>(); BAR();
    vmwait<0>(); BAR();
  }
  BAR();
  float M;
  {
    const LAS float* mx = (const LAS float*)(L + AL_MAX) + l32;
    M = fmaxf(fmaxf(mx[0], mx[32]), fmaxf(mx[64], mx[96]));
  }
  if (w < 4) {
    float sum = 0.f;
#pragma unroll
    for (int k = 0; k < 16; ++k) { S[k] = __builtin_amdgcn_exp2f(S[k] - M); sum += S[k]; }
    sum = x32_sum(sum);
    if (lane < 32) *(LAS float*)(L + AL_SUM + (w * 32 + lane) * 4) = sum;
    const u32 sw = swz(l32);
#pragma unroll
    for (int qd = 0; qd < 4; ++qd) {
      const v2u o = {pk_bf16(S[4 * qd], S[4 * qd + 1]), pk_bf16(S[4 * qd + 2], S[4 * qd + 3])};
      *(LAS v2u*)(L + AL_P + l32 * 256 + (((4 * w + qd) ^ sw) << 4) + 8 * hh) = o;
    }
  }
  BAR();
  STAMP(4);
  f32x16 O0, O1;
#pragma unroll
  for (int k = 0; k < 16; ++k) { O0[k] = 0.f; O1[k] = 0.f; }
  {
    const LAS char* tile = L + AL_CKV + (w >> 1) * 32768;
    const int blk = (lane >> 4) & 1, q = (lane & 15) >> 2, pp = lane & 3;
    const u32 sw = swz(l32);
    const u32 c0 = 8 * (w & 1) + 2 * blk + (pp >> 1);
#pragma unroll
    for (int ks = 0; ks < 8; ++ks) {
      const v4u pb = ldsr128(L + AL_P + l32 * 256 + (((2 * ks + hh) ^ sw) << 4));
      const u32 r0 = 16 * ks + 8 * hh + q, r1 = r0 + 4;
      const u32 s0 = swz(r0), s1 = swz(r1);
      const v4u a0 = tr2(tile + 256 * r0 + 16 * (c0 ^ s0) + 8 * (pp & 1), tile + 256 * r1 + 16 * (c0 ^ s1) + 8 * (pp & 1));
      const v4u a1 = tr2(tile + 256 * r0 + 16 * ((c0 + 4) ^ s0) + 8 * (pp & 1), tile + 256 * r1 + 16 * ((c0 + 4) ^ s1) + 8 * (pp & 1));
      O0 = mfma32(a0, pb, O0);
      O1 = mfma32(a1, pb, O1);
    }
  }
  float Lsum = 0.f;
  if (w == 0) { const LAS float* sm = (const LAS float*)(L + AL_SUM) + l32; Lsum = (sm[0] + sm[32]) + (sm[64] + sm[96]); }
  BAR();
  STAMP(5);
#pragma unroll
  for (int qd = 0; qd < 4; ++qd) {
    const v2u o0 = {canon(pk_bf16(O0[4 * qd], O0[4 * qd + 1])), canon(pk_bf16(O0[4 * qd + 2], O0[4 * qd + 3]))};
    const v2u o1 = {canon(pk_bf16(O1[4 * qd], O1[4 * qd + 1])), canon(pk_bf16(O1[4 * qd + 2], O1[4 * qd + 3]))};
    *(LAS v2u*)(L + AL_O + l32 * 1024 + (((8 * w + qd) ^ (l32 & 15)) << 4) + 8 * hh) = o0;
    *(LAS v2u*)(L + AL_O + l32 * 1024 + (((8 * w + 4 + qd) ^ (l32 & 15)) << 4) + 8 * hh) = o1;
  }
  if (w == 0 && lane < 32) { const v2u s2 = {canonf(M), canonf(Lsum)}; wst64(ws, WS_ST + ((lane * 2 + b) * 64 + kc) * 8, s2); }
  BAR();
#pragma unroll
  for (int k = 0; k < 4; ++k) {
    const int hd = 4 * w + k;
    const v4u v = ldsr128(L + AL_O + hd * 1024 + ((lane ^ (hd & 15)) << 4));
    wst128(ws, WS_S4 + ((((hd * 4 + g) * 2 + b) * 64 + kc) * 256) + i16 * 16, v);
  }
  STAMP(6);
  const bool do_mod = j >= 48, do_ffn = j < 108;
  if (do_mod) {
    const char* sm = (const char*)P.wmod + (size_t)(1280 + 32 * (j - 48)) * 2;
    const u32 vo = (lane >> 2) * 7680 + (lane & 3) * 16;
    for (int n = w; n < 40; n += 8) dma_s(AL_MW + n * 1024, sm + (size_t)n * 16 * 7680, vo);
  }
  if (do_ffn) {
    const char* gp = (const char*)(((lane & 2) ? P.wu : P.wg)) + (size_t)(lane >> 2) * 3456 + j * 32 + (lane & 1) * 16;
    for (int n = w; n < 40; n += 8) dma_v(AL_GU + n * 1024, gp + (size_t)n * 16 * 3456);
    const char* sd = (const char*)P.wd + (size_t)(16 * j) * 1280;
    for (int n = w; n < 20; n += 8) dma_s(AL_DN + n * 1024, sd + n * 1024, lane * 16);
  }
  if (do_mod) {
    if (tid < 160) *(LAS v4u*)(L + AL_SB + tid * 16) = sv;
    if (do_ffn) { if (w < 4) vmwait<8>(); else vmwait<7>(); } else vmwait<0>();
    BAR();
    gemv640(L, AL_MW, AL_SB, w, lane);
    BAR();
    if (w == 0 && lane < 16) {
      const LAS f32x4* wr = (const LAS f32x4*)(L + AL_RED + lane * 16);
      f32x4 v = wr[0];
#pragma unroll
      for (int k = 1; k < 8; ++k) v += wr[16 * k];
      const v4u oo = {canonf(v[0]), canonf(v[1]), canonf(v[2]), canonf(v[3])};
      const int col = 32 * (j - 48);
      wst128(ws, WS_MB + (((col / 640) * 2 + (lane >> 3)) * 640 + col % 640) * 4 + (lane & 7) * 16, oo);
    }
    STAMP(7);
  }
  if (!do_ffn) return;
  {
    const int bb = w >> 2, m0 = 160 * (w & 3) + 4 * (lane < 40 ? lane : 0);
    const v2u xv = *(const GAS v2u*)(P.x + bb * 640 + m0);
    f32x4 x1, sbv, cbv;
    for (u32 sp = 0; sp < SPIN_CAP; ++sp) {
      CBAR();
      v4u t[7];
#pragma unroll
      for (int k = 0; k < 4; ++k) t[k] = wld128(ws, WS_S6 + ((k * 2 + bb) * 640 + m0) * 4);
#pragma unroll
      for (int k = 0; k < 3; ++k) t[4 + k] = wld128(ws, WS_MB + ((k * 2 + bb) * 640 + m0) * 4);
      bool ok = true;
#pragma unroll
      for (int k = 0; k < 7; ++k) ok = ok && ok4(t[k]);
      const f32x4 at = (__builtin_bit_cast(f32x4, t[0]) + __builtin_bit_cast(f32x4, t[1])) + (__builtin_bit_cast(f32x4, t[2]) + __builtin_bit_cast(f32x4, t[3]));
      const f32x4 xf = {bflo(xv.x), bfhi(xv.x), bflo(xv.y), bfhi(xv.y)};
      x1 = xf + __builtin_bit_cast(f32x4, t[4]) * at;
      sbv = __builtin_bit_cast(f32x4, t[5]); cbv = __builtin_bit_cast(f32x4, t[6]);
      if (__all(ok)) break;
      __builtin_amdgcn_s_sleep(1);
    }
    STAMP(8);
    const bool act = lane < 40;
    const float mw = wave_sum(act ? (x1[0] + x1[1]) + (x1[2] + x1[3]) : 0.f) * (1.f / 160.f);
    const float d0 = x1[0] - mw, d1 = x1[1] - mw, d2 = x1[2] - mw, d3 = x1[3] - mw;
    const float m2 = wave_sum(act ? (d0 * d0 + d1 * d1) + (d2 * d2 + d3 * d3) : 0.f);
    if (lane == 0) { LAS f32x2* sp = (LAS f32x2*)(L + AL_STAT); sp[w] = f32x2{mw, m2}; }
    BAR();
    const LAS f32x4* sp = (const LAS f32x4*)(L + AL_STAT + bb * 32);
    const f32x4 s01 = sp[0], s23 = sp[1];
    const float mean = ((s01[0] + s01[2]) + (s23[0] + s23[2])) * 0.25f;
    const float e0 = s01[0] - mean, e1 = s01[2] - mean, e2 = s23[0] - mean, e3 = s23[2] - mean;
    const float var = (((s01[1] + s01[3]) + (s23[1] + s23[3])) + 160.f * ((e0 * e0 + e1 * e1) + (e2 * e2 + e3 * e3))) * (1.f / 640.f);
    const float rstd = rsqrtf(var + 1e-5f);
    float hv[4];
#pragma unroll
    for (int k = 0; k < 4; ++k) hv[k] = (x1[k] - mean) * rstd * (1.f + cbv[k]) + sbv[k];
    if (act) { const v2u o = {pk_bf16(hv[0], hv[1]), pk_bf16(hv[2], hv[3])}; *(LAS v2u*)(L + AL_H2 + (bb * 640 + m0) * 2) = o; }
  }
  BAR();
  gemv640(L, AL_GU, AL_H2, w, lane);
  BAR();
  if (w == 0) {
    const int bb = (lane >> 4) & 1, f = lane & 15;
    const LAS float* wr = (const LAS float*)(L + AL_RED) + bb * 32 + f;
    float gs = 0.f, us = 0.f;
#pragma unroll
    for (int k = 0; k < 8; ++k) { gs += wr[64 * k]; us += wr[64 * k + 16]; }
    const float a = silu_f(gs) * us;
    const u32 ab = pk_bf16(a, 0.f) & 0xFFFFu;
    LAS u16* ac = (LAS u16*)(L + AL_ACT);
    if (lane < 32) ac[bb * 32 + f] = (u16)ab; else ac[bb * 32 + 16 + f] = 0;
  }
  BAR();
  STAMP(9);
  {
    const v4u bq = ldsr128(L + AL_ACT + (lane & 1) * 64 + g * 16);
    const int q = (lane & 15) >> 2, pp = lane & 3;
    const int r0 = (8 * g + q) & 15;
#pragma unroll
    for (int k = 0; k < 5; ++k) {
      const int tile = 5 * w + k;
      const LAS char* a = L + AL_DN + r0 * 1280 + tile * 32 + 8 * pp;
      const v4u fa = tr2(a, a + 4 * 1280);
      f32x4 acc = {0.f, 0.f, 0.f, 0.f};
      acc = mfma16(fa, bq, acc);
      if (i16 < 2) { const v4u o = {canonf(acc[0]), canonf(acc[1]), canonf(acc[2]), canonf(acc[3])};
        wst128(ws, WS_S8 + tile * (108 * 128) + j * 128 + i16 * 64 + g * 16, o); }
    }
  }
  STAMP(10);
}

__global__ void __launch_bounds__(512, 2)
fused_decode(const u16* x, const u16* ckv, const u16* wq, const u16* wuk, const u16* wuv, const u16* wo, const u16* wg,
             const u16* wu, const u16* wd, const u16* c, const u16* wmod, u16* out, char* wsp) {
  extern __shared__ __attribute__((aligned(1024))) char lds[];
  LAS char* L = (LAS char*)lds;
  const int lane = threadIdx.x & 63, w = __builtin_amdgcn_readfirstlane(threadIdx.x >> 6);
  const rsrc_t ws = __builtin_amdgcn_make_buffer_rsrc((void*)wsp, 0, WS_END, 0x00020000);
  Ptrs P{x, ckv, wq, wuk, wuv, wo, wg, wu, wd, c, wmod, out, wsp};
  if (blockIdx.x < 128) f_role(P, L, ws, blockIdx.x, w, lane);
  else a_role(P, L, ws, blockIdx.x - 128, w, lane);
}

extern "C" void kernel_launch(void* const* d_in, const int* in_sizes, int n_in, void* d_out, int out_size, void* d_ws,
                              size_t ws_size, hipStream_t stream) {
  (void)in_sizes; (void)n_in; (void)out_size; (void)ws_size;
  hipFuncSetAttribute((const void*)fused_decode, hipFuncAttributeMaxDynamicSharedMemorySize, LDS_BYTES);
  fused_decode<<<256, 512, LDS_BYTES, stream>>>((const u16*)d_in[0], (const u16*)d_in[1], (const u16*)d_in[2], (const u16*)d_in[3],
                                                (const u16*)d_in[4], (const u16*)d_in[5], (const u16*)d_in[7], (const u16*)d_in[8],
                                                (const u16*)d_in[9], (const u16*)d_in[10], (const u16*)d_in[11], (u16*)d_out, (char*)d_ws);
}
```

```cpp
#include <hip/hip_runtime.h>
#include <hip/hip_bf16.h>
#include <stdint.h>

typedef unsigned short u16;
typedef unsigned int u32;
typedef unsigned long long u64;
typedef u32 v4u __attribute__((ext_vector_type(4)));
typedef u32 v2u __attribute__((ext_vector_type(2)));
typedef float f32x4 __attribute__((ext_vector_type(4)));
typedef float f32x2 __attribute__((ext_vector_type(2)));
typedef float f32x16 __attribute__((ext_vector_type(16)));
typedef short s16x4 __attribute__((ext_vector_type(4)));
typedef short s16x8 __attribute__((ext_vector_type(8)));
typedef __bf16 bf16x8 __attribute__((ext_vector_type(8)));
typedef __bf16 bf16x2 __attribute__((ext_vector_type(2)));
#define GAS __attribute__((address_space(1)))
#define LAS __attribute__((address_space(3)))
#define DI __device__ __forceinline__

#define A_DELAY 0
#define F_TAIL_DELAY 0

#define CODE_PF 16384
constexpr u32 SENT = 0xFFFFFFFFu;
constexpr u32 SPIN_CAP = 1u << 17;

constexpr int WS_EPOCH = 0;
constexpr int WS_FLAGF = 256;
constexpr int WS_S1 = WS_FLAGF + 128 * 128 + 3840;
constexpr int WS_S2 = WS_S1 + 5 * 4 * 640 * 4;
constexpr int WS_S3 = WS_S2 + 32 * 4 * 1024;
constexpr int WS_S5 = WS_S3 + 2 * 32 * 512 * 2;
constexpr int WS_S6 = WS_S5 + 32 * 4 * 1024;
constexpr int WS_MB = WS_S6 + 8 * 2 * 640 * 4;
constexpr int WS_S8 = WS_MB + 4 * 2 * 640 * 4;
constexpr int WS_ST = WS_S8 + 40 * 108 * 32 * 4;
constexpr int WS_DN = WS_ST + 128 * 8 * 128;
constexpr int WS_S4 = WS_DN + 128 * 8 * 128;
constexpr int WS_END = WS_S4 + 32 * 4 * 2 * 64 * 256;
constexpr int WS_STAMP = WS_END;
static_assert(WS_S1 % 256 == 0 && WS_S2 % 256 == 0 && WS_S3 % 256 == 0 && WS_S5 % 256 == 0 && WS_S6 % 256 == 0 && WS_MB % 256 == 0 &&
              WS_S8 % 256 == 0 && WS_ST % 256 == 0 && WS_S4 % 256 == 0, "align");

constexpr int LDS_BYTES = 163840;

typedef __amdgpu_buffer_rsrc_t rsrc_t;
DI v4u wld128(rsrc_t r, int off) { return __builtin_amdgcn_raw_buffer_load_b128(r, off, 0, 16); }
DI v2u wld64(rsrc_t r, int off) { return __builtin_amdgcn_raw_buffer_load_b64(r, off, 0, 16); }
DI u32 wld32(rsrc_t r, int off) { return __builtin_amdgcn_raw_buffer_load_b32(r, off, 0, 16); }
DI void wst128(rsrc_t r, int off, v4u v) { __builtin_amdgcn_raw_buffer_store_b128(v, r, off, 0, 16); }
DI void wst64(rsrc_t r, int off, v2u v) { __builtin_amdgcn_raw_buffer_store_b64(v, r, off, 0, 16); }
DI void wst32(rsrc_t r, int off, u32 v) { __builtin_amdgcn_raw_buffer_store_b32(v, r, off, 0, 16); }
DI u32 canon(u32 x) { return x < 0xFFFFFFFEu ? x : 0xFFFFFFFEu; }
DI bool ok4(v4u v) { return v.x != SENT && v.y != SENT && v.z != SENT && v.w != SENT; }
DI v4u tag4(f32x4 v, u32 T) {
  v4u o;
  o.x = __builtin_amdgcn_perm(__float_as_uint(v[0]), T, 0x07060500u); o.y = __builtin_amdgcn_perm(__float_as_uint(v[1]), T, 0x07060501u);
  o.z = __builtin_amdgcn_perm(__float_as_uint(v[2]), T, 0x07060502u); o.w = __builtin_amdgcn_perm(__float_as_uint(v[3]), T, 0x07060503u);
  return o;
}
DI bool tok4(v4u v, u32 T) { return (__builtin_amdgcn_perm(v.y, v.x, 0x0c0c0400u) | __builtin_amdgcn_perm(v.w, v.z, 0x04000c0cu)) == T; }
DI u32 tbad4(v4u v, u32 T) { return (__builtin_amdgcn_perm(v.y, v.x, 0x0c0c0400u) | __builtin_amdgcn_perm(v.w, v.z, 0x04000c0cu)) ^ T; }
constexpr int OOB = 0x40000000;
DI f32x4 asf4(v4u v) { return __builtin_bit_cast(f32x4, v); }
DI u32 mk_tag(u32 epoch_word) { return (epoch_word + 1u) * 0x01010101u; }
#define VM0() do { __builtin_amdgcn_s_waitcnt(0x0F70); asm volatile("" ::: "memory"); } while (0)
DI float bflo(u32 x) { return __uint_as_float(x << 16); }
DI float bfhi(u32 x) { return __uint_as_float(x & 0xffff0000u); }
DI u32 pk_bf16(float lo, float hi) { f32x2 f = {lo, hi}; bf16x2 h = __builtin_convertvector(f, bf16x2); return __builtin_bit_cast(u32, h); }
DI float silu_f(float x) { return x * __builtin_amdgcn_rcpf(1.f + __expf(-x)); }
DI v4u ldnt128(const void* p) { return __builtin_nontemporal_load((const GAS v4u*)p); }
DI f32x4 mfma16(v4u a, v4u b, f32x4 c) { return __builtin_amdgcn_mfma_f32_16x16x32_bf16(__builtin_bit_cast(bf16x8, a), __builtin_bit_cast(bf16x8, b), c, 0, 0, 0); }
DI f32x16 mfma32(v4u a, v4u b, f32x16 c) { return __builtin_amdgcn_mfma_f32_32x32x16_bf16(__builtin_bit_cast(bf16x8, a), __builtin_bit_cast(bf16x8, b), c, 0, 0, 0); }
#define BAR() asm volatile("s_waitcnt lgkmcnt(0)\n\ts_barrier" ::: "memory")
#define CBAR() asm volatile("" ::: "memory")
template <int N> DI void vmwait() { asm volatile("s_waitcnt vmcnt(%0)" :: "i"(N) : "memory"); }

template <int CTRL> DI float dpp(float x) { return __builtin_bit_cast(float, __builtin_amdgcn_mov_dpp(__builtin_bit_cast(int, x), CTRL, 0xf, 0xf, true)); }
DI float x16_sum(float x) { auto s = __builtin_amdgcn_permlane16_swap(__float_as_uint(x), __float_as_uint(x), false, false); return __uint_as_float(s[0]) + __uint_as_float(s[1]); }
DI float x32_sum(float x) { auto s = __builtin_amdgcn_permlane32_swap(__float_as_uint(x), __float_as_uint(x), false, false); return __uint_as_float(s[0]) + __uint_as_float(s[1]); }
DI float x16_max(float x) { auto s = __builtin_amdgcn_permlane16_swap(__float_as_uint(x), __float_as_uint(x), false, false); return fmaxf(__uint_as_float(s[0]), __uint_as_float(s[1])); }
DI float x32_max(float x) { auto s = __builtin_amdgcn_permlane32_swap(__float_as_uint(x), __float_as_uint(x), false, false); return fmaxf(__uint_as_float(s[0]), __uint_as_float(s[1])); }
DI float wave_sum(float x) { x += dpp<0xB1>(x); x += dpp<0x4E>(x); x += dpp<0x141>(x); x += dpp<0x140>(x); return x32_sum(x16_sum(x)); }

DI void dma_s(u32 ldsaddr, const void* sbase, u32 voff) {
  u32 keep;
  asm volatile("s_mov_b32 %0, m0\n\ts_mov_b32 m0, %1\n\ts_nop 0\n\tglobal_load_lds_dwordx4 %2, %3 nt\n\ts_mov_b32 m0, %0"
               : "=&s"(keep) : "s"(ldsaddr), "v"(voff), "s"(sbase) : "memory");
}
DI void dma_s_keep(u32 ldsaddr, const void* sbase, u32 voff) {
  u32 keep;
  asm volatile("s_mov_b32 %0, m0\n\ts_mov_b32 m0, %1\n\ts_nop 0\n\tglobal_load_lds_dwordx4 %2, %3\n\ts_mov_b32 m0, %0"
               : "=&s"(keep) : "s"(ldsaddr), "v"(voff), "s"(sbase) : "memory");
}
DI void dma_v(u32 ldsaddr, const void* vaddr) {
  u32 keep;
  asm volatile("s_mov_b32 %0, m0\n\ts_mov_b32 m0, %1\n\ts_nop 0\n\tglobal_load_lds_dwordx4 %2, off nt\n\ts_mov_b32 m0, %0"
               : "=&s"(keep) : "s"(ldsaddr), "v"(vaddr) : "memory");
}
DI u32 swz(u32 row) { return ((row & 3u) << 2) | ((row >> 2) & 3u); }
DI v4u ldsr128(const LAS char* p) { return *(const LAS v4u*)p; }
DI v4u tr2(const LAS char* p0, const LAS char* p1) {
  s16x4 a = __builtin_amdgcn_ds_read_tr16_b64_v4i16((LAS s16x4*)p0);
  s16x4 b = __builtin_amdgcn_ds_read_tr16_b64_v4i16((LAS s16x4*)p1);
  s16x8 v = __builtin_shufflevector(a, b, 0, 1, 2, 3, 4, 5, 6, 7);
  return __builtin_bit_cast(v4u, v);
}
DI void wait_ticks(u64 t0, u32 ticks) { if (ticks) { while (__builtin_amdgcn_s_memrealtime() - t0 < ticks) __builtin_amdgcn_s_sleep(2); } }

#define STAMP(i) do { } while (0)
#define STARG

struct Ptrs { const u16 *x, *ckv, *wq, *wuk, *wuv, *wo, *wg, *wu, *wd, *c, *wmod; u16* out; char* ws; const char* code; u64 t0; };

DI v4u frag64(const LAS char* img, int lane, int c, int ks) {
  const int g = lane >> 4, q = (lane & 15) >> 2, p = lane & 3;
  const LAS char* a = img + (32 * ks + 8 * g + q) * 64 + 32 * c + 8 * p;
  return tr2(a, a + 4 * 64);
}
DI v4u frag256(const LAS char* img, int lane, int c, int ks) {
  const int g = lane >> 4, q = (lane & 15) >> 2, p = lane & 3;
  const u32 r0 = 8 * g + q, r1 = r0 + 4, ch = 2 * c + (p >> 1);
  return tr2(img + 8192 * ks + 256 * r0 + 16 * (ch ^ swz(r0)) + 8 * (p & 1), img + 8192 * ks + 256 * r1 + 16 * (ch ^ swz(r1)) + 8 * (p & 1));
}
template <int N> DI f32x4 gather_sum(rsrc_t ws, int off, int stride, u32 T) {
  f32x4 s;
  _Pragma("nounroll") for (u32 sp = 0; sp < SPIN_CAP; ++sp) {
    CBAR();
    v4u t[N];
#pragma unroll
    for (int k = 0; k < N; ++k) t[k] = wld128(ws, off + k * stride);
    u32 bad = 0;
#pragma unroll
    for (int k = 0; k < N; ++k) bad |= tbad4(t[k], T);
    s = asf4(t[0]);
#pragma unroll
    for (int k = 1; k < N; ++k) s += asf4(t[k]);
    if (__all(bad == 0)) break;
  }
  return s;
}
template <int SLEEP> DI void wait_piece(rsrc_t ws, int off, u32 T, int lane) {
  _Pragma("nounroll") for (u32 sp = 0; sp < SPIN_CAP; ++sp) {
    CBAR();
    const v4u f = wld128(ws, lane == 0 ? off : OOB);
    if (__builtin_amdgcn_readfirstlane(tbad4(f, T)) == 0) break;
    __builtin_amdgcn_s_sleep(SLEEP);
  }
}
DI u32 lds_ld(const LAS char* p) { return *(const volatile LAS u32*)p; }
#define LGKM0() asm volatile("s_waitcnt lgkmcnt(0)" ::: "memory")

constexpr int FL_WQ = 0, FL_WUK = 40960, FL_WO = 0, FL_WUV = 81920, FL_SM = FL_WUV + 32768;
constexpr int FL_HB = FL_SM, FL_QB = FL_HB + 1024, FL_QA = FL_QB + 512, FL_CRED = FL_QA + 512, FL_CST = FL_CRED + 4096,
              FL_CB = FL_CST + 64, FL_CTX = FL_CB + 512, FL_WRED = FL_CTX + 4096, FL_FIN = FL_WRED + 4096, FL_TAG = FL_FIN + 1024, FL_PF = FL_TAG + 64,
              FL_SB = FL_PF + 1024  ;
static_assert(FL_PF % 16 == 0 && FL_SB + 2560 <= LDS_BYTES, "lds");

DI void f_role(const Ptrs& P, LAS char* L, rsrc_t ws, int p, int w, int lane STARG) {
  const int i16 = lane & 15, g = lane >> 4;
  const int h = p & 31, sl = p >> 5;
  const int un = (p & 7) + 8 * (p >> 4), sub = (p >> 3) & 1;
  const f32x4 Z4 = {0.f, 0.f, 0.f, 0.f};
  float ln[4] = {0.f, 0.f, 0.f, 0.f};
  if (w < 2) {
    const v4u xa = *(const GAS v4u*)(P.x + w * 640 + 8 * lane);
    const v4u xb = *(const GAS v4u*)(P.x + w * 640 + 512 + 8 * (lane & 15));
    const v2u xs = *(const GAS v2u*)(P.x + w * 640 + 160 * sl + 4 * (lane < 40 ? lane : 0));
    float v[16];
#pragma unroll
    for (int k = 0; k < 4; ++k) { v[2 * k] = bflo(xa[k]); v[2 * k + 1] = bfhi(xa[k]); v[8 + 2 * k] = bflo(xb[k]); v[9 + 2 * k] = bfhi(xb[k]); }
    const bool ex = lane < 16;
    float s = 0.f;
#pragma unroll
    for (int k = 0; k < 8; ++k) s += v[k] + (ex ? v[8 + k] : 0.f);
    const float mean = wave_sum(s) * (1.f / 640.f);
    float s2 = 0.f;
#pragma unroll
    for (int k = 0; k < 8; ++k) { const float d = v[k] - mean, e = v[8 + k] - mean; s2 += d * d + (ex ? e * e : 0.f); }
    const float rstd = rsqrtf(wave_sum(s2) * (1.f / 640.f) + 1e-5f);
    ln[0] = (bflo(xs.x) - mean) * rstd; ln[1] = (bfhi(xs.x) - mean) * rstd; ln[2] = (bflo(xs.y) - mean) * rstd; ln[3] = (bfhi(xs.y) - mean) * rstd;
  } else if (w < 6) {
    const char* sb = (const char*)P.wq + (size_t)(160 * sl) * 8192 + h * 256;
    const u32 n0 = w - 2;
    const u32 vo = g * 8192 + ((i16 ^ ((g << 2) | n0)) << 4);
#pragma unroll 2
    for (int k = 0; k < 10; ++k) dma_s(FL_WQ + (n0 + 4 * k) * 1024, sb + (size_t)(n0 + 4 * k) * 4 * 8192, vo);
    const char* su = (const char*)P.wuk + (size_t)(128 * sl) * 8192 + h * 256;
#pragma unroll 2
    for (int k = 0; k < 8; ++k) dma_s(FL_WUK + (n0 + 4 * k) * 1024, su + (size_t)(n0 + 4 * k) * 4 * 8192, vo);
    vmwait<8>();
  } else if (w == 7) {
    const u32 T0 = mk_tag(wld32(ws, WS_EPOCH));
    if (lane == 0) *(LAS u32*)(L + FL_TAG) = T0;
    {
      const int o0 = (p >> 3) * 1024;
      dma_s_keep(FL_PF, P.code + (o0 < CODE_PF - 1024 ? o0 : CODE_PF - 1024), lane * 16);
      dma_s_keep(FL_PF, P.code + (o0 + 16384 < CODE_PF - 1024 ? o0 + 16384 : CODE_PF - 1024), lane * 16);
    }
    const v4u S = {SENT, SENT, SENT, SENT};
    wst128(ws, lane < 32 ? WS_S3 + ((lane >> 4) * 32 + h) * 1024 + sl * 256 + i16 * 16 : OOB, S);
  }
  STAMP(1);
  BAR();
  const u32 T = __builtin_amdgcn_readfirstlane(lds_ld(L + FL_TAG));
  v4u aq[5];
#pragma unroll
  for (int k = 0; k < 5; ++k) aq[k] = frag256(L + FL_WQ, lane, w, k);
  STAMP(2);
  if (w < 2) {
    f32x4 sa, ca;
    _Pragma("nounroll") for (u32 sp = 0; sp < SPIN_CAP; ++sp) {
      CBAR();
      v4u t[10];
#pragma unroll
      for (int k = 0; k < 10; ++k) t[k] = wld128(ws, lane < 40 ? WS_S1 + (((k >> 1) * 4 + (k & 1) * 2 + w) * 640 + 160 * sl + 4 * lane) * 4 : OOB);
      u32 bad = 0;
#pragma unroll
      for (int k = 0; k < 10; ++k) bad |= tbad4(t[k], T);
      if (lane >= 40) bad = 0;
      sa = asf4(t[0]); ca = asf4(t[1]);
#pragma unroll
      for (int k = 1; k < 5; ++k) { sa += asf4(t[2 * k]); ca += asf4(t[2 * k + 1]); }
      if (__all(bad == 0)) break;
    }
    const float h0 = ln[0] * (1.f + ca[0]) + sa[0], h1 = ln[1] * (1.f + ca[1]) + sa[1];
    const float h2 = ln[2] * (1.f + ca[2]) + sa[2], h3 = ln[3] * (1.f + ca[3]) + sa[3];
    if (lane < 40) { const v2u o = {pk_bf16(h0, h1), pk_bf16(h2, h3)}; *(LAS v2u*)(L + FL_HB + w * 320 + lane * 8) = o; }
    STAMP(4);
  } else if (w < 6) {
    vmwait<0>();
  } else if (w == 7) {
    VM0();
    const v2u f = {T, ~T};
    wst64(ws, lane == 0 ? WS_FLAGF + p * 128 : OOB, f);
  }
  BAR();
  v4u au[4];
  {
    v4u bh[5];
#pragma unroll
    for (int k = 0; k < 5; ++k) bh[k] = ldsr128(L + FL_HB + (lane & 1) * 320 + (32 * k + 8 * g) * 2);
    const u32 row = 16 * w + i16, sw = swz(row);
#pragma unroll
    for (int k = 0; k < 4; ++k) au[k] = ldsr128(L + FL_WUK + row * 256 + (((4 * k + g) ^ sw) << 4));
    f32x4 acc = Z4;
#pragma unroll
    for (int k = 0; k < 5; ++k) acc = mfma16(aq[k], bh[k], acc);
    wst128(ws, i16 < 2 ? WS_S2 + (h * 4 + sl) * 1024 + w * 128 + i16 * 64 + g * 16 : OOB, tag4(acc, T));
  }
  STAMP(5);
  if (w == 0) {
    const f32x4 q = gather_sum<4>(ws, WS_S2 + h * 4096 + lane * 16, 1024, T);
    const v2u o = {pk_bf16(q[0], q[1]), pk_bf16(q[2], q[3])};
    *(LAS v2u*)(L + FL_QB + ((lane >> 2) & 1) * 256 + (16 * (lane >> 3) + 4 * (lane & 3)) * 2) = o;
    STAMP(6);
  }
  BAR();
  {
    v4u bq[4];
#pragma unroll
    for (int k = 0; k < 4; ++k) bq[k] = ldsr128(L + FL_QB + (lane & 1) * 256 + (32 * k + 8 * g) * 2);
    f32x4 acc = Z4;
#pragma unroll
    for (int k = 0; k < 4; ++k) acc = mfma16(au[k], bq[k], acc);
    constexpr float SC = 0.08838834764831845f * 1.4426950408889634f;
    if (i16 < 2) { const v2u o = {canon(pk_bf16(acc[0] * SC, acc[1] * SC)), canon(pk_bf16(acc[2] * SC, acc[3] * SC))};
      *(LAS v2u*)(L + FL_QA + i16 * 256 + (16 * w + 4 * g) * 2) = o; }
  }
  BAR();
  if (w == 0) wst128(ws, lane < 32 ? WS_S3 + ((lane >> 4) * 32 + h) * 1024 + sl * 256 + i16 * 16 : OOB, ldsr128(L + FL_QA + (lane & 31) * 16));
  STAMP(7);
  const int kq = p / 10, ms = p % 10;
  if (w >= 4) {
    wait_ticks(P.t0, F_TAIL_DELAY);
    const u32 n0 = w - 4;
    const char* sb = (const char*)P.wuv + (size_t)(128 * sl) * 8192 + h * 256;
    const u32 vo = g * 8192 + ((i16 ^ ((g << 2) | n0)) << 4);
#pragma unroll 2
    for (int k = 0; k < 8; ++k) dma_s(FL_WUV + (n0 + 4 * k) * 1024, sb + (size_t)(n0 + 4 * k) * 4 * 8192, vo);
    if (p < 80) {
      const char* so = (const char*)P.wo + (size_t)(512 * kq) * 1280 + ms * 128;
      const u32 vo2 = (lane >> 3) * 1280 + (lane & 7) * 16;
#pragma unroll 2
      for (int n = n0; n < 64; n += 4) dma_s(FL_WO + n * 1024, so + (size_t)n * 8 * 1280, vo2);
    } else if (p >= 88) {
      const char* sm = (const char*)P.wmod + (size_t)(1280 + 64 * (p - 88)) * 2;
      const u32 vo2 = (lane >> 3) * 7680 + (lane & 7) * 16;
#pragma unroll 2
      for (int n = n0; n < 80; n += 4) dma_s(FL_WO + n * 1024, sm + (size_t)n * 8 * 7680, vo2);
    }
    vmwait<0>();
  } else if (p >= 88 && w < 3) {
    const int tid = w * 64 + lane;
    const v4u cvv = *(const GAS v4u*)(P.c + (tid < 160 ? tid : 0) * 8);
    v4u sv;
#pragma unroll
    for (int k = 0; k < 4; ++k) sv[k] = pk_bf16(silu_f(bflo(cvv[k])), silu_f(bfhi(cvv[k])));
    if (tid < 160) *(LAS v4u*)(L + FL_SB + tid * 16) = sv;
  }
  BAR();
  STAMP(8);
  if (p >= 88) {
    f32x4 a[4] = {Z4, Z4, Z4, Z4};
    const int q = (lane & 15) >> 2, pp = lane & 3;
    for (int ks = w; ks < 20; ks += 8) {
      const v4u b = ldsr128(L + FL_SB + (lane & 1) * 1280 + (32 * ks + 8 * g) * 2);
      const LAS char* base = L + FL_WO + (32 * ks + 8 * g + q) * 128 + 8 * pp;
#pragma unroll
      for (int c = 0; c < 4; ++c) a[c] = mfma16(tr2(base + 32 * c, base + 32 * c + 4 * 128), b, a[c]);
    }
    if (i16 < 2) {
      LAS f32x4* wr = (LAS f32x4*)(L + FL_WRED + w * 512 + i16 * 256 + g * 16);
#pragma unroll
      for (int c = 0; c < 4; ++c) wr[4 * c] = a[c];
    }
    BAR();
    if (w == 0) {
      const LAS f32x4* wr = (const LAS f32x4*)(L + FL_WRED + (lane & 31) * 16);
      f32x4 v = wr[0];
#pragma unroll
      for (int k = 1; k < 8; ++k) v += wr[32 * k];
      const int col = 64 * (p - 88);
      wst128(ws, lane < 32 ? WS_MB + (((col / 640) * 2 + (lane >> 4)) * 640 + col % 640) * 4 + (lane & 15) * 16 : OOB, tag4(v, T));
    }
  }
  v4u uv[4], ao[8];
#pragma unroll
  for (int k = 0; k < 4; ++k) uv[k] = frag256(L + FL_WUV, lane, w, k);
  if (p < 80) {
    const LAS char* base = L + FL_WO + (64 * w + 8 * g + ((lane & 15) >> 2)) * 128 + 8 * (lane & 3);
#pragma unroll
    for (int k = 0; k < 8; ++k) { const LAS char* a = base + (k >> 2) * 32 * 128 + (k & 3) * 32; ao[k] = tr2(a, a + 4 * 128); }
  }
  {
    const int b = w >> 2, k0 = 16 * (w & 3);
    const int sto = WS_ST + ((b * 64 + k0 + i16) * 8 + (h & 7)) * 128 + (h >> 4) * 16;
    if (w == 0) wait_piece<8>(ws, sto, T, lane);
    BAR();
    v4u s;
    _Pragma("nounroll") for (u32 sp = 0; sp < SPIN_CAP; ++sp) {
      CBAR();
      s = wld128(ws, lane < 16 ? sto : OOB);
      if (__all(lane >= 16 || tbad4(s, T) == 0)) break;
      __builtin_amdgcn_s_sleep(2);
    }
    float wj[4];
    {
      const bool odd = (h >> 3) & 1;
      const float m = __uint_as_float((odd ? s.z : s.x) & 0xFFFFFF00u), l = __uint_as_float(odd ? s.w : s.y);
      float Mw = fmaxf(m, dpp<0xB1>(m)); Mw = fmaxf(Mw, dpp<0x4E>(Mw)); Mw = fmaxf(Mw, dpp<0x141>(Mw)); Mw = fmaxf(Mw, dpp<0x140>(Mw));
      const float e = __builtin_amdgcn_exp2f(m - Mw);
      float Lw = e * l; Lw += dpp<0xB1>(Lw); Lw += dpp<0x4E>(Lw); Lw += dpp<0x141>(Lw); Lw += dpp<0x140>(Lw);
      if (lane == 0) *(LAS f32x2*)(L + FL_CST + w * 8) = f32x2{Mw, Lw};
#pragma unroll
      for (int j = 0; j < 4; ++j) wj[j] = __shfl(e, 4 * j + g);
    }
    STAMP(9);
    _Pragma("nounroll") for (u32 sp = 0; sp < SPIN_CAP; ++sp) {
      CBAR();
      const v4u f = wld128(ws, lane < 16 ? WS_DN + ((b * 64 + k0 + i16) * 8 + (h >> 3)) * 128 : OOB);
      if (__all(lane >= 16 || tbad4(f, T) == 0)) break;
    }
    CBAR();
    STAMP(3);
    v4u t[4];
#pragma unroll
    for (int j = 0; j < 4; ++j) t[j] = wld128(ws, WS_S4 + ((((h * 4 + sl) * 2 + b) * 64 + k0 + 4 * j) * 256) + lane * 16);
    float acc[8];
#pragma unroll
    for (int k = 0; k < 4; ++k) { acc[2 * k] = wj[0] * bflo(t[0][k]); acc[2 * k + 1] = wj[0] * bfhi(t[0][k]); }
#pragma unroll
    for (int j = 1; j < 4; ++j) {
#pragma unroll
      for (int k = 0; k < 4; ++k) { acc[2 * k] += wj[j] * bflo(t[j][k]); acc[2 * k + 1] += wj[j] * bfhi(t[j][k]); }
    }
#pragma unroll
    for (int k = 0; k < 8; ++k) acc[k] = x32_sum(x16_sum(acc[k]));
    if (lane < 16) {
      LAS f32x4* cr = (LAS f32x4*)(L + FL_CRED + w * 512 + lane * 32);
      cr[0] = f32x4{acc[0], acc[1], acc[2], acc[3]}; cr[1] = f32x4{acc[4], acc[5], acc[6], acc[7]};
    }
    BAR();
    if (w == 0) {
      const int bb = lane >> 5;
      const LAS f32x4* cs = (const LAS f32x4*)(L + FL_CST + bb * 32);
      const f32x4 c01 = cs[0], c23 = cs[1];
      const LAS f32x4* cr = (const LAS f32x4*)(L + FL_CRED + bb * 2048 + (lane & 31) * 16);
      const f32x4 r0 = cr[0], r1 = cr[32], r2 = cr[64], r3 = cr[96];
      const float M = fmaxf(fmaxf(c01[0], c01[2]), fmaxf(c23[0], c23[2]));
      const float f0 = __builtin_amdgcn_exp2f(c01[0] - M), f1 = __builtin_amdgcn_exp2f(c01[2] - M), f2 = __builtin_amdgcn_exp2f(c23[0] - M), f3 = __builtin_amdgcn_exp2f(c23[2] - M);
      const float inv = __builtin_amdgcn_rcpf((f0 * c01[1] + f1 * c01[3]) + (f2 * c23[1] + f3 * c23[3]));
      const f32x4 v = ((r0 * f0 + r1 * f1) + (r2 * f2 + r3 * f3)) * inv;
      const v2u o = {pk_bf16(v[0], v[1]), pk_bf16(v[2], v[3])};
      *(LAS v2u*)(L + FL_CB + lane * 8) = o;
    }
    BAR();
    v4u bc[4];
#pragma unroll
    for (int k = 0; k < 4; ++k) bc[k] = ldsr128(L + FL_CB + (lane & 1) * 256 + (32 * k + 8 * g) * 2);
    f32x4 a2 = Z4;
#pragma unroll
    for (int k = 0; k < 4; ++k) a2 = mfma16(uv[k], bc[k], a2);
    wst128(ws, i16 < 2 ? WS_S5 + (h * 4 + sl) * 1024 + w * 128 + i16 * 64 + g * 16 : OOB, tag4(a2, T));
  }
  STAMP(10);
  if (p < 80) {
    f32x4 cx = gather_sum<2>(ws, WS_S5 + (4 * kq + (w >> 1)) * 4096 + (lane >> 5) * 1024 + (w & 1) * 512 + (lane & 31) * 16, 2048, T);
    STAMP(11);
#pragma unroll
    for (int k = 0; k < 4; ++k) cx[k] = x32_sum(cx[k]);
    const v2u o = {pk_bf16(cx[0], cx[1]), pk_bf16(cx[2], cx[3])};
    if (lane < 32) *(LAS v2u*)(L + FL_CTX + w * 256 + ((lane >> 2) & 1) * 128 + (16 * (lane >> 3) + 4 * (lane & 3)) * 2) = o;
    LGKM0();
    v4u bx[2];
#pragma unroll
    for (int k = 0; k < 2; ++k) bx[k] = ldsr128(L + FL_CTX + w * 256 + (lane & 1) * 128 + (32 * k + 8 * g) * 2);
    f32x4 a[4];
#pragma unroll
    for (int c = 0; c < 4; ++c) a[c] = mfma16(ao[4 + c], bx[1], mfma16(ao[c], bx[0], Z4));
    if (i16 < 2) {
      LAS f32x4* wr = (LAS f32x4*)(L + FL_WRED + w * 512 + i16 * 256 + g * 16);
#pragma unroll
      for (int c = 0; c < 4; ++c) wr[4 * c] = a[c];
    }
    BAR();
    if (w == 0) {
      const LAS f32x4* wr = (const LAS f32x4*)(L + FL_WRED + (lane & 31) * 16);
      f32x4 v[8];
#pragma unroll
      for (int k = 0; k < 8; ++k) v[k] = wr[32 * k];
      const f32x4 r = ((v[0] + v[1]) + (v[2] + v[3])) + ((v[4] + v[5]) + (v[6] + v[7]));
      wst128(ws, lane < 32 ? WS_S6 + ((kq * 2 + (lane >> 4)) * 640 + 64 * ms) * 4 + (lane & 15) * 16 : OOB, tag4(r, T));
    }
    STAMP(12);
  } else if (p >= 88) {
    const int fs = p - 88, bm = lane & 7, bb = bm >> 2, m0 = 16 * fs + 4 * (bm & 3);
    f32x4 x1, gb;
    if (w == 0) {
      const v2u xv = *(const GAS v2u*)(P.x + bb * 640 + m0);
      _Pragma("nounroll") for (u32 sp = 0; sp < SPIN_CAP; ++sp) {
        CBAR();
        const v4u t0 = wld128(ws, WS_S6 + (((lane >> 3) * 2 + bb) * 640 + m0) * 4);
        const v4u t4 = wld128(ws, lane < 8 ? WS_MB + ((0 * 2 + bb) * 640 + m0) * 4 : OOB);
        const v4u t5 = wld128(ws, lane < 8 ? WS_MB + ((3 * 2 + bb) * 640 + m0) * 4 : OOB);
        const u32 bad = tbad4(t0, T) | (lane < 8 ? tbad4(t4, T) | tbad4(t5, T) : 0u);
        f32x4 at = asf4(t0);
#pragma unroll
        for (int k = 0; k < 4; ++k) { float v = at[k]; v += dpp<0x128>(v); at[k] = x32_sum(x16_sum(v)); }
        const f32x4 xf = {bflo(xv.x), bfhi(xv.x), bflo(xv.y), bfhi(xv.y)};
        x1 = xf + asf4(t4) * at;
        gb = asf4(t5);
        if (__all(bad == 0)) break;
        __builtin_amdgcn_s_sleep(4);
      }
    }
    BAR();
    STAMP(11);
    f32x4 y;
    const int tid = w * 64 + lane;
    _Pragma("nounroll") for (u32 sp = 0; sp < SPIN_CAP; ++sp) {
      CBAR();
      const v4u t0 = wld128(ws, WS_S8 + fs * (108 * 128) + tid * 16);
      const v4u t1 = wld128(ws, WS_S8 + fs * (108 * 128) + (tid < 352 ? 512 + tid : tid) * 16);
      y = asf4(t0);
      if (tid < 352) y += asf4(t1);
      if (__all((tbad4(t0, T) | tbad4(t1, T)) == 0)) break;
    }
    STAMP(12);
#pragma unroll
    for (int k = 0; k < 4; ++k) { float v = y[k]; v += dpp<0x128>(v); y[k] = x32_sum(x16_sum(v)); }
    if (lane < 8) *(LAS f32x4*)(L + FL_FIN + w * 128 + lane * 16) = y;
    BAR();
    if (w == 0) {
      const LAS f32x4* fr = (const LAS f32x4*)(L + FL_FIN + bm * 16);
      f32x4 v[8];
#pragma unroll
      for (int k = 0; k < 8; ++k) v[k] = fr[8 * k];
      const f32x4 o = x1 + gb * (((v[0] + v[1]) + (v[2] + v[3])) + ((v[4] + v[5]) + (v[6] + v[7])));
      const v2u ob = {pk_bf16(o[0], o[1]), pk_bf16(o[2], o[3])};
      if (lane < 8) *(GAS v2u*)(P.out + bb * 640 + m0) = ob;
      if (p == 88 && lane == 0) wst32(ws, WS_EPOCH, wld32(ws, WS_EPOCH) + 1u);
    }
    STAMP(13);
  }
}

constexpr int AL_CKV = 0, AL_Q = 131072;
constexpr int AL_P = AL_Q, AL_MAX = AL_Q + 8192, AL_SUM = AL_MAX + 512;
constexpr int AL_O = 0, AL_GU = 32768, AL_DN = AL_Q + 10240  , AL_SM = AL_GU + 40960;
constexpr int AL_H2 = AL_SM, AL_STAT = AL_H2 + 2560, AL_RED = AL_STAT + 64, AL_ACT = AL_RED + 1024;
static_assert(AL_ACT + 8 * 128 <= AL_Q && AL_SUM + 512 <= AL_DN && AL_DN + 20480 <= LDS_BYTES, "lds");

DI u32 p1_phase(const Ptrs& P, LAS char* L, rsrc_t ws, int j, int w, int lane) {
  const int i16 = lane & 15, g = lane >> 4, l32 = lane & 31;
  const f32x4 Z4 = {0.f, 0.f, 0.f, 0.f};
  u32 T = wld32(ws, WS_EPOCH);
  {
    const int un = (j & 7) + 8 * (j >> 4), cg = 2 * (un / 5) + ((j >> 3) & 1), ks1 = un % 5;
    if (un < 50) {
      const int base = 128 * ks1 + 16 * w;
      const int bb = (lane >> 3) & 1, r1 = i16 >> 3, mat = (i16 >> 2) & 1, ch = i16 & 3, jj = lane & 7;
      u32 cv[2]; v4u wm[2];
#pragma unroll
      for (int t = 0; t < 2; ++t) cv[t] = *(const GAS u32*)(P.c + bb * 640 + base + 8 * t + 2 * g);
#pragma unroll
      for (int t = 0; t < 2; ++t) wm[t] = ldnt128(P.wmod + (size_t)(base + 8 * t + 2 * g + r1) * 3840 + mat * 640 + 32 * cg + 8 * ch);
      u32 msk[4];
#pragma unroll
      for (int r = 0; r < 4; ++r) msk[r] = (r == (jj >> 1)) ? (0xFFFFu << (16 * (jj & 1))) : 0u;
      f32x4 a0 = Z4, a1 = Z4;
#pragma unroll
      for (int t = 0; t < 2; ++t) {
        const u32 sp = pk_bf16(silu_f(bflo(cv[t])), silu_f(bfhi(cv[t])));
        const u32 d0 = (sp & 0xFFFFu) * 0x10001u, d1 = (sp >> 16) * 0x10001u;
        const v4u B0 = {d0 & msk[0], d0 & msk[1], d0 & msk[2], d0 & msk[3]};
        const v4u B1 = {d1 & msk[0], d1 & msk[1], d1 & msk[2], d1 & msk[3]};
        a0 = mfma16(wm[t], B0, a0);
        a1 = mfma16(wm[t], B1, a1);
      }
      LAS float* red = (LAS float*)(L + AL_Q) + w * 128;
      const int mt = (lane >> 4) & 1;
#pragma unroll
      for (int r = 0; r < 4; ++r) {
        const float v = x32_sum(lane < 32 ? a0[r] : a1[r]);
        if (lane < 32) red[(mt * 2 + bb) * 32 + 8 * r + jj] = v;
      }
      T = __builtin_amdgcn_readfirstlane(mk_tag(T));
      BAR();
      if (w == 0) {
        const LAS f32x4* rd = (const LAS f32x4*)(L + AL_Q) + l32;
        const f32x4 s = ((rd[0] + rd[32]) + (rd[64] + rd[96])) + ((rd[128] + rd[160]) + (rd[192] + rd[224]));
        wst128(ws, lane < 32 ? WS_S1 + ((ks1 * 4 + (lane >> 3)) * 640 + 32 * cg) * 4 + (lane & 7) * 16 : OOB, tag4(s, T));
      }
    } else {
      T = __builtin_amdgcn_readfirstlane(mk_tag(T));
    }
  }
  return T;
}

DI void a_role(const Ptrs& P, LAS char* L, rsrc_t ws, int j, int w, int lane, u32 T STARG) {
  const int i16 = lane & 15, g = lane >> 4, l32 = lane & 31, hh = lane >> 5;
  const int b = j >> 6, kc = j & 63;
  const f32x4 Z4 = {0.f, 0.f, 0.f, 0.f};
  f32x16 S;
  const int fl = (j & 7) + 8 * (j >> 5), fsub = (j >> 3) & 3, fj = 4 * fl + fsub;
  const bool do_ffn = fl < 27;
  v4u dummy = {0u, 0u, 0u, 0u};
  STAMP(14);
  if (w >= 4) {
    const int lw = w - 4;
    const char* sb = (const char*)P.ckv + ((size_t)(b * 8192 + kc * 128 + 32 * lw)) * 1024;
    u32 vo[4];
#pragma unroll
    for (int i = 0; i < 4; ++i) vo[i] = g * 1024 + ((i16 ^ ((g << 2) | i)) << 4);
    wait_ticks(P.t0, A_DELAY);
    for (int cq = 0; cq < 4; ++cq) {
#pragma unroll
      for (int i = 0; i < 8; ++i) dma_s(AL_CKV + cq * 32768 + (8 * lw + i) * 1024, sb + i * 4096 + cq * 256, vo[i & 3]);
    }
    STAMP(1);
    vmwait<16>();
  } else {
    _Pragma("nounroll") for (u32 sp = 0; sp < SPIN_CAP; ++sp) {
      CBAR();
      const v2u f = wld64(ws, lane < 32 ? WS_FLAGF + (8 * w + (lane & 7) + 32 * (lane >> 3)) * 128 : OOB);
      if (__all(lane >= 32 || ((f.x ^ T) | (f.y ^ ~T)) == 0)) break;
      __builtin_amdgcn_s_sleep(16);
    }
    STAMP(1);
    _Pragma("nounroll") for (u32 sp = 0; sp < SPIN_CAP; ++sp) {
      CBAR();
      const u32 f = wld32(ws, lane == 0 ? WS_S3 + (b * 32 + 8 * w) * 1024 : OOB);
      if (__builtin_amdgcn_readfirstlane(f) != SENT) break;
      __builtin_amdgcn_s_sleep(4);
    }
    _Pragma("nounroll") for (u32 sp = 0; sp < SPIN_CAP; ++sp) {
      CBAR();
      v4u t[8];
#pragma unroll
      for (int k = 0; k < 8; ++k) t[k] = wld128(ws, WS_S3 + (b * 32 + 8 * w + k) * 1024 + lane * 16);
      u32 mx = 0;
#pragma unroll
      for (int k = 0; k < 8; ++k) { const u32 a = max(max(t[k].x, t[k].y), max(t[k].z, t[k].w)); mx = max(mx, a); }
      if (__all(mx != SENT)) {
#pragma unroll
        for (int k = 0; k < 8; ++k) { const int hd = 8 * w + k; *(LAS v4u*)(L + AL_Q + hd * 1024 + ((lane ^ (hd & 15)) << 4)) = t[k]; }
        break;
      }
      __builtin_amdgcn_s_sleep(2);
    }
    STAMP(2);
  }
  BAR();
  if (w < 4) {
#pragma unroll
    for (int k = 0; k < 16; ++k) S[k] = 0.f;
    const u32 row = 32 * w + l32, sw = swz(row);
    u32 ca[8], qa[8];
#pragma unroll
    for (int s = 0; s < 8; ++s) { ca[s] = AL_CKV + row * 256 + (((2 * s + hh) ^ sw) << 4); qa[s] = AL_Q + l32 * 1024 + (((2 * s + hh) ^ (l32 & 15)) << 4); }
#pragma unroll
    for (int hf = 0; hf < 2; ++hf) {
      if (hf > 0) BAR();
      v4u a[16], q[16];
#pragma unroll
      for (int s = 0; s < 16; ++s) {
        a[s] = ldsr128(L + ca[s & 7] + hf * 65536 + (s >> 3) * 32768);
        q[s] = ldsr128(L + qa[s & 7] + hf * 512 + (s >> 3) * 256);
      }
      __builtin_amdgcn_sched_barrier(0);
#pragma unroll
      for (int s = 0; s < 16; ++s) S = mfma32(a[s], q[s], S);
    }
    STAMP(3);
    float m0 = fmaxf(fmaxf(S[0], S[1]), fmaxf(S[2], S[3])), m1 = fmaxf(fmaxf(S[4], S[5]), fmaxf(S[6], S[7]));
    float m2 = fmaxf(fmaxf(S[8], S[9]), fmaxf(S[10], S[11])), m3 = fmaxf(fmaxf(S[12], S[13]), fmaxf(S[14], S[15]));
    const float m = x32_max(fmaxf(fmaxf(m0, m1), fmaxf(m2, m3)));
    if (lane < 32) *(LAS float*)(L + AL_MAX + (w * 32 + lane) * 4) = m;
  } else {
    vmwait<0>();
    BAR();
    if (do_ffn) {
      const int lw = w - 4;
#pragma unroll 2
      for (int n = lw; n < 40; n += 4) {
        const int line = n * 8 + (lane >> 3), mat = line >= 160, row = 160 * fsub + line - 160 * mat;
        const char* pp = (const char*)(mat ? P.wu : P.wg) + (size_t)row * 3456 + fl * 128 + (lane & 7) * 16;
        asm volatile("global_load_dwordx4 %0, %1, off" : "+v"(dummy) : "v"(pp) : "memory");
      }
    }
  }
  BAR();
  float M;
  {
    const LAS float* mx = (const LAS float*)(L + AL_MAX) + l32;
    M = fmaxf(fmaxf(mx[0], mx[32]), fmaxf(mx[64], mx[96]));
    M = __uint_as_float(__float_as_uint(M) & 0xFFFFFF00u);
  }
  if (w < 4) {
    float sum = 0.f;
#pragma unroll
    for (int k = 0; k < 16; ++k) { S[k] = __builtin_amdgcn_exp2f(S[k] - M); sum += S[k]; }
    const u32 sw = swz(l32);
#pragma unroll
    for (int qd = 0; qd < 4; ++qd) {
      const v2u o = {pk_bf16(S[4 * qd], S[4 * qd + 1]), pk_bf16(S[4 * qd + 2], S[4 * qd + 3])};
      *(LAS v2u*)(L + AL_P + l32 * 256 + (((4 * w + qd) ^ sw) << 4) + 8 * hh) = o;
    }
    sum = x32_sum(sum);
    if (lane < 32) *(LAS float*)(L + AL_SUM + (w * 32 + lane) * 4) = sum;
  }
  BAR();
  STAMP(4);
  if (w == 7) {
    const LAS float* sm = (const LAS float*)(L + AL_SUM) + l32;
    const float Ls = (sm[0] + sm[32]) + (sm[64] + sm[96]);
    const float M2 = dpp<0x128>(M), L2 = dpp<0x128>(Ls);
    wst128(ws, (lane < 32 && !(lane & 8)) ? WS_ST + ((b * 64 + kc) * 8 + (lane & 7)) * 128 + (lane >> 4) * 16 : OOB, tag4(f32x4{M, Ls, M2, L2}, T));
  }
  f32x16 O0, O1;
#pragma unroll
  for (int k = 0; k < 16; ++k) { O0[k] = 0.f; O1[k] = 0.f; }
  {
    const int blk = (lane >> 4) & 1, q = (lane & 15) >> 2, pp = lane & 3;
    const u32 sw = swz(l32);
    const u32 c0 = 8 * (w & 1) + 2 * blk + (pp >> 1);
    const u32 r0 = 8 * hh + q, r1 = r0 + 4, s0 = swz(r0), s1 = swz(r1);
    const u32 tb = AL_CKV + (w >> 1) * 32768 + 8 * (pp & 1);
    const u32 a00 = tb + 256 * r0 + 16 * (c0 ^ s0), a01 = tb + 256 * r1 + 16 * (c0 ^ s1);
    const u32 a10 = tb + 256 * r0 + 16 * ((c0 + 4) ^ s0), a11 = tb + 256 * r1 + 16 * ((c0 + 4) ^ s1);
    v4u pb[8], a0[8], a1[8];
#pragma unroll
    for (int ks = 0; ks < 8; ++ks) {
      pb[ks] = ldsr128(L + AL_P + l32 * 256 + (((2 * ks + hh) ^ sw) << 4));
      a0[ks] = tr2(L + a00 + ks * 4096, L + a01 + ks * 4096);
      a1[ks] = tr2(L + a10 + ks * 4096, L + a11 + ks * 4096);
    }
    __builtin_amdgcn_sched_barrier(0);
#pragma unroll
    for (int ks = 0; ks < 8; ++ks) { O0 = mfma32(a0[ks], pb[ks], O0); O1 = mfma32(a1[ks], pb[ks], O1); }
  }
  BAR();
  STAMP(5);
#pragma unroll
  for (int qd = 0; qd < 4; ++qd) {
    const v2u o0 = {pk_bf16(O0[4 * qd], O0[4 * qd + 1]), pk_bf16(O0[4 * qd + 2], O0[4 * qd + 3])};
    const v2u o1 = {pk_bf16(O1[4 * qd], O1[4 * qd + 1]), pk_bf16(O1[4 * qd + 2], O1[4 * qd + 3])};
    *(LAS v2u*)(L + AL_O + l32 * 1024 + (((8 * w + qd) ^ (l32 & 15)) << 4) + 8 * hh) = o0;
    *(LAS v2u*)(L + AL_O + l32 * 1024 + (((8 * w + 4 + qd) ^ (l32 & 15)) << 4) + 8 * hh) = o1;
  }
  BAR();
  if (w < 4) {
    v4u v[8];
#pragma unroll
    for (int k = 0; k < 8; ++k) { const int hd = 8 * w + k; v[k] = ldsr128(L + AL_O + hd * 1024 + ((lane ^ (hd & 15)) << 4)); }
#pragma unroll
    for (int k = 0; k < 8; ++k) wst128(ws, WS_S4 + (((((8 * w + k) * 4 + g) * 2 + b) * 64 + kc) * 256) + i16 * 16, v[k]);
    VM0();
    wst128(ws, lane == 0 ? WS_DN + ((b * 64 + kc) * 8 + w) * 128 : OOB, tag4(Z4, T));
  } else {
    vmwait<0>();
  }
  STAMP(6);
  asm volatile("" :: "v"(dummy));
  if (!do_ffn) return;
  const int bb = w >> 2, m0 = 160 * (w & 3) + 4 * (lane < 40 ? lane : 0);
  const v2u xv = *(const GAS v2u*)(P.x + bb * 640 + m0);
  v4u mb[3];
  _Pragma("nounroll") for (u32 sp = 0; sp < SPIN_CAP; ++sp) {
    CBAR();
#pragma unroll
    for (int k = 0; k < 3; ++k) mb[k] = wld128(ws, lane < 40 ? WS_MB + ((k * 2 + bb) * 640 + m0) * 4 : OOB);
    if (__all(lane >= 40 || (tbad4(mb[0], T) | tbad4(mb[1], T) | tbad4(mb[2], T)) == 0)) break;
    __builtin_amdgcn_s_sleep(8);
  }
  BAR();
  {
    const char* gp = (const char*)(((lane & 2) ? P.wu : P.wg)) + (size_t)(lane >> 2) * 3456 + fj * 32 + (lane & 1) * 16;
    for (int n = w; n < 40; n += 8) dma_v(AL_GU + n * 1024, gp + (size_t)n * 16 * 3456);
    const char* sd = (const char*)P.wd + (size_t)(16 * fj) * 1280;
    for (int n = w; n < 20; n += 8) dma_s(AL_DN + n * 1024, sd + n * 1024, lane * 16);
    vmwait<0>();
  }
  BAR();
  v4u ag[5], ad[5];
  {
#pragma unroll
    for (int i = 0; i < 5; ++i) ag[i] = frag64(L + AL_GU, lane, w & 1, (w >> 1) + 4 * i);
    const int q = (lane & 15) >> 2, pp = lane & 3;
    const int r0 = (8 * g + q) & 15;
#pragma unroll
    for (int k = 0; k < 5; ++k) { const LAS char* a = L + AL_DN + r0 * 1280 + (5 * w + k) * 32 + 8 * pp; ad[k] = tr2(a, a + 4 * 1280); }
  }
  STAMP(7);
  {
    wait_piece<6>(ws, WS_S5 + w * 4096 * 4 + 3 * 1024, T, lane);
    f32x4 at;
    _Pragma("nounroll") for (u32 sp = 0; sp < SPIN_CAP; ++sp) {
      CBAR();
      v4u t[8];
#pragma unroll
      for (int k = 0; k < 8; ++k) t[k] = wld128(ws, lane < 40 ? WS_S6 + ((k * 2 + bb) * 640 + m0) * 4 : OOB);
      at = ((asf4(t[0]) + asf4(t[1])) + (asf4(t[2]) + asf4(t[3]))) + ((asf4(t[4]) + asf4(t[5])) + (asf4(t[6]) + asf4(t[7])));
      u32 bad = 0;
#pragma unroll
      for (int k = 0; k < 8; ++k) bad |= tbad4(t[k], T);
      if (__all(lane >= 40 || bad == 0)) break;
    }
    const f32x4 xf = {bflo(xv.x), bfhi(xv.x), bflo(xv.y), bfhi(xv.y)};
    const f32x4 x1 = xf + asf4(mb[0]) * at, sbv = asf4(mb[1]), cbv = asf4(mb[2]);
    STAMP(8);
    const bool act = lane < 40;
    const float pv = __builtin_bit_cast(float, __builtin_amdgcn_readfirstlane(__builtin_bit_cast(int, x1[0])));
    const float d0 = x1[0] - pv, d1 = x1[1] - pv, d2 = x1[2] - pv, d3 = x1[3] - pv;
    const float s1 = wave_sum(act ? (d0 + d1) + (d2 + d3) : 0.f);
    const float s2 = wave_sum(act ? (d0 * d0 + d1 * d1) + (d2 * d2 + d3 * d3) : 0.f);
    const float mw = pv + s1 * (1.f / 160.f), m2 = s2 - s1 * s1 * (1.f / 160.f);
    if (lane == 0) { LAS f32x2* sp = (LAS f32x2*)(L + AL_STAT); sp[w] = f32x2{mw, m2}; }
    BAR();
    const LAS f32x4* sp = (const LAS f32x4*)(L + AL_STAT + bb * 32);
    const f32x4 s01 = sp[0], s23 = sp[1];
    const float mean = ((s01[0] + s01[2]) + (s23[0] + s23[2])) * 0.25f;
    const float e0 = s01[0] - mean, e1 = s01[2] - mean, e2 = s23[0] - mean, e3 = s23[2] - mean;
    const float var = (((s01[1] + s01[3]) + (s23[1] + s23[3])) + 160.f * ((e0 * e0 + e1 * e1) + (e2 * e2 + e3 * e3))) * (1.f / 640.f);
    const float rstd = rsqrtf(var + 1e-5f);
    float hv[4];
#pragma unroll
    for (int k = 0; k < 4; ++k) hv[k] = (x1[k] - mean) * rstd * (1.f + cbv[k]) + sbv[k];
    if (act) { const v2u o = {pk_bf16(hv[0], hv[1]), pk_bf16(hv[2], hv[3])}; *(LAS v2u*)(L + AL_H2 + (bb * 640 + m0) * 2) = o; }
  }
  BAR();
  {
    v4u bh[5];
#pragma unroll
    for (int i = 0; i < 5; ++i) bh[i] = ldsr128(L + AL_H2 + (lane & 1) * 1280 + (32 * ((w >> 1) + 4 * i) + 8 * g) * 2);
    f32x4 a = Z4;
#pragma unroll
    for (int i = 0; i < 5; ++i) a = mfma16(ag[i], bh[i], a);
    if (i16 < 2) *(LAS f32x4*)(L + AL_RED + w * 128 + i16 * 64 + g * 16) = a;
  }
  BAR();
  {
    const int b2 = (lane >> 4) & 1, f = lane & 15;
    const LAS float* wr = (const LAS float*)(L + AL_RED) + b2 * 16 + f;
    const float gs = (wr[0] + wr[64]) + (wr[128] + wr[192]), us = (wr[32] + wr[96]) + (wr[160] + wr[224]);
    const float a = silu_f(gs) * us;
    LAS u16* ac = (LAS u16*)(L + AL_ACT + w * 128);
    ac[b2 * 32 + (lane >> 5) * 16 + f] = lane < 32 ? (u16)(pk_bf16(a, 0.f) & 0xFFFFu) : (u16)0;
    LGKM0();
    STAMP(9);
    const v4u bq = ldsr128(L + AL_ACT + w * 128 + (lane & 1) * 64 + g * 16);
    f32x4 acc[5];
#pragma unroll
    for (int k = 0; k < 5; ++k) acc[k] = mfma16(ad[k], bq, Z4);
#pragma unroll
    for (int k = 0; k < 5; ++k) wst128(ws, i16 < 2 ? WS_S8 + (5 * w + k) * (108 * 128) + fj * 128 + i16 * 64 + g * 16 : OOB, tag4(acc[k], T));
  }
  STAMP(10);
}

__global__ void __launch_bounds__(512, 2)
fused_decode(const u16* x, const u16* ckv, const u16* wq, const u16* wuk, const u16* wuv, const u16* wo, const u16* wg,
             const u16* wu, const u16* wd, const u16* c, const u16* wmod, u16* out, char* wsp) {
  extern __shared__ __attribute__((aligned(1024))) char lds[];
  LAS char* L = (LAS char*)lds;
  const int lane = threadIdx.x & 63, w = __builtin_amdgcn_readfirstlane(threadIdx.x >> 6);
  const rsrc_t ws = __builtin_amdgcn_make_buffer_rsrc((void*)wsp, 0, WS_END, 0x00020000);
  u64 pc;
  asm volatile("s_getpc_b64 %0" : "=s"(pc));
  Ptrs P{x, ckv, wq, wuk, wuv, wo, wg, wu, wd, c, wmod, out, wsp, (const char*)(pc & ~255ull), __builtin_amdgcn_s_memrealtime()};
  u32 T = 0;
  if (blockIdx.x >= 128) T = p1_phase(P, L, ws, blockIdx.x - 128, w, lane);
  if (blockIdx.x < 128) f_role(P, L, ws, blockIdx.x, w, lane);
  else a_role(P, L, ws, blockIdx.x - 128, w, lane, T);
}

extern "C" void kernel_launch(void* const* d_in, const int* in_sizes, int n_in, void* d_out, int out_size, void* d_ws,
                              size_t ws_size, hipStream_t stream) {
  (void)in_sizes; (void)n_in; (void)out_size; (void)ws_size;
  (void)hipFuncSetAttribute((const void*)fused_decode, hipFuncAttributeMaxDynamicSharedMemorySize, LDS_BYTES);
  fused_decode<<<256, 512, LDS_BYTES, stream>>>((const u16*)d_in[0], (const u16*)d_in[1], (const u16*)d_in[2], (const u16*)d_in[3],
                                                (const u16*)d_in[4], (const u16*)d_in[5], (const u16*)d_in[7], (const u16*)d_in[8],
                                                (const u16*)d_in[9], (const u16*)d_in[10], (const u16*)d_in[11], (u16*)d_out, (char*)d_ws);
}
```

```cpp
#include <hip/hip_runtime.h>
#include <hip/hip_bf16.h>
#include <stdint.h>

typedef unsigned short u16;
typedef unsigned int u32;
typedef unsigned long long u64;
typedef u32 v4u __attribute__((ext_vector_type(4)));
typedef u32 v2u __attribute__((ext_vector_type(2)));
typedef float f32x4 __attribute__((ext_vector_type(4)));
typedef float f32x2 __attribute__((ext_vector_type(2)));
typedef float f32x16 __attribute__((ext_vector_type(16)));
typedef short s16x4 __attribute__((ext_vector_type(4)));
typedef short s16x8 __attribute__((ext_vector_type(8)));
typedef __bf16 bf16x8 __attribute__((ext_vector_type(8)));
typedef __bf16 bf16x2 __attribute__((ext_vector_type(2)));
#define GAS __attribute__((address_space(1)))
#define LAS __attribute__((address_space(3)))
#define DI __device__ __forceinline__

#define A_DELAY 0
#define F_TAIL_DELAY 0

#define CODE_PF 24576
constexpr u32 SENT = 0xFFFFFFFFu;
constexpr u32 SPIN_CAP = 1u << 17;

constexpr int WS_EPOCH = 0;
constexpr int WS_FLAGF = 256;
constexpr int WS_S1 = WS_FLAGF + 128 * 128 + 3840;
constexpr int WS_S2 = WS_S1 + 5 * 4 * 640 * 4;
constexpr int WS_S3 = WS_S2 + 32 * 4 * 1024;
constexpr int WS_S5 = WS_S3 + 2 * 32 * 512 * 2;
constexpr int WS_S6 = WS_S5 + 32 * 4 * 1024;
constexpr int WS_MB = WS_S6 + 8 * 2 * 640 * 4;
constexpr int WS_S8 = WS_MB + 4 * 2 * 640 * 4;
constexpr int WS_ST = WS_S8 + 40 * 108 * 32 * 4;
constexpr int WS_DN = WS_ST + 128 * 8 * 128;
constexpr int WS_S4 = WS_DN + 128 * 8 * 128;
constexpr int WS_END = WS_S4 + 32 * 4 * 2 * 64 * 256;
constexpr int WS_STAMP = WS_END;
static_assert(WS_S1 % 256 == 0 && WS_S2 % 256 == 0 && WS_S3 % 256 == 0 && WS_S5 % 256 == 0 && WS_S6 % 256 == 0 && WS_MB % 256 == 0 &&
              WS_S8 % 256 == 0 && WS_ST % 256 == 0 && WS_S4 % 256 == 0, "align");

constexpr int LDS_BYTES = 163840;

typedef __amdgpu_buffer_rsrc_t rsrc_t;
DI v4u wld128(rsrc_t r, int off) { return __builtin_amdgcn_raw_buffer_load_b128(r, off, 0, 16); }
DI v2u wld64(rsrc_t r, int off) { return __builtin_amdgcn_raw_buffer_load_b64(r, off, 0, 16); }
DI u32 wld32(rsrc_t r, int off) { return __builtin_amdgcn_raw_buffer_load_b32(r, off, 0, 16); }
DI void wst128(rsrc_t r, int off, v4u v) { __builtin_amdgcn_raw_buffer_store_b128(v, r, off, 0, 16); }
DI void wst64(rsrc_t r, int off, v2u v) { __builtin_amdgcn_raw_buffer_store_b64(v, r, off, 0, 16); }
DI void wst32(rsrc_t r, int off, u32 v) { __builtin_amdgcn_raw_buffer_store_b32(v, r, off, 0, 16); }
DI u32 canon(u32 x) { return x < 0xFFFFFFFEu ? x : 0xFFFFFFFEu; }
DI bool ok4(v4u v) { return v.x != SENT && v.y != SENT && v.z != SENT && v.w != SENT; }
DI v4u tag4(f32x4 v, u32 T) {
  v4u o;
  o.x = __builtin_amdgcn_perm(__float_as_uint(v[0]), T, 0x07060500u); o.y = __builtin_amdgcn_perm(__float_as_uint(v[1]), T, 0x07060501u);
  o.z = __builtin_amdgcn_perm(__float_as_uint(v[2]), T, 0x07060502u); o.w = __builtin_amdgcn_perm(__float_as_uint(v[3]), T, 0x07060503u);
  return o;
}
DI bool tok4(v4u v, u32 T) { return (__builtin_amdgcn_perm(v.y, v.x, 0x0c0c0400u) | __builtin_amdgcn_perm(v.w, v.z, 0x04000c0cu)) == T; }
DI u32 tbad4(v4u v, u32 T) { return (__builtin_amdgcn_perm(v.y, v.x, 0x0c0c0400u) | __builtin_amdgcn_perm(v.w, v.z, 0x04000c0cu)) ^ T; }
constexpr int OOB = 0x40000000;
DI f32x4 asf4(v4u v) { const v4u m = v & 0xFFFFFF00u; return __builtin_bit_cast(f32x4, m); }
DI u32 mk_tag(u32 epoch_word) { return (epoch_word + 1u) * 0x01010101u; }
#define VM0() do { __builtin_amdgcn_s_waitcnt(0x0F70); asm volatile("" ::: "memory"); } while (0)
DI float bflo(u32 x) { return __uint_as_float(x << 16); }
DI float bfhi(u32 x) { return __uint_as_float(x & 0xffff0000u); }
DI u32 pk_bf16(float lo, float hi) { f32x2 f = {lo, hi}; bf16x2 h = __builtin_convertvector(f, bf16x2); return __builtin_bit_cast(u32, h); }
DI float silu_f(float x) { return x * __builtin_amdgcn_rcpf(1.f + __expf(-x)); }
DI v4u ldnt128(const void* p) { return __builtin_nontemporal_load((const GAS v4u*)p); }
DI f32x4 mfma16(v4u a, v4u b, f32x4 c) { return __builtin_amdgcn_mfma_f32_16x16x32_bf16(__builtin_bit_cast(bf16x8, a), __builtin_bit_cast(bf16x8, b), c, 0, 0, 0); }
DI f32x16 mfma32(v4u a, v4u b, f32x16 c) { return __builtin_amdgcn_mfma_f32_32x32x16_bf16(__builtin_bit_cast(bf16x8, a), __builtin_bit_cast(bf16x8, b), c, 0, 0, 0); }
#define BAR() asm volatile("s_waitcnt lgkmcnt(0)\n\ts_barrier" ::: "memory")
#define CBAR() asm volatile("" ::: "memory")
template <int N> DI void vmwait() { asm volatile("s_waitcnt vmcnt(%0)" :: "i"(N) : "memory"); }

template <int CTRL> DI float dpp(float x) { return __builtin_bit_cast(float, __builtin_amdgcn_mov_dpp(__builtin_bit_cast(int, x), CTRL, 0xf, 0xf, true)); }
DI float x16_sum(float x) { auto s = __builtin_amdgcn_permlane16_swap(__float_as_uint(x), __float_as_uint(x), false, false); return __uint_as_float(s[0]) + __uint_as_float(s[1]); }
DI float x32_sum(float x) { auto s = __builtin_amdgcn_permlane32_swap(__float_as_uint(x), __float_as_uint(x), false, false); return __uint_as_float(s[0]) + __uint_as_float(s[1]); }
DI float x16_max(float x) { auto s = __builtin_amdgcn_permlane16_swap(__float_as_uint(x), __float_as_uint(x), false, false); return fmaxf(__uint_as_float(s[0]), __uint_as_float(s[1])); }
DI float x32_max(float x) { auto s = __builtin_amdgcn_permlane32_swap(__float_as_uint(x), __float_as_uint(x), false, false); return fmaxf(__uint_as_float(s[0]), __uint_as_float(s[1])); }
DI float wave_sum(float x) { x += dpp<0xB1>(x); x += dpp<0x4E>(x); x += dpp<0x141>(x); x += dpp<0x140>(x); return x32_sum(x16_sum(x)); }

DI void dma_s(u32 ldsaddr, const void* sbase, u32 voff) {
  u32 keep;
  asm volatile("s_mov_b32 %0, m0\n\ts_mov_b32 m0, %1\n\ts_nop 0\n\tglobal_load_lds_dwordx4 %2, %3 nt\n\ts_mov_b32 m0, %0"
               : "=&s"(keep) : "s"(ldsaddr), "v"(voff), "s"(sbase) : "memory");
}
DI void dma_s_keep(u32 ldsaddr, const void* sbase, u32 voff) {
  u32 keep;
  asm volatile("s_mov_b32 %0, m0\n\ts_mov_b32 m0, %1\n\ts_nop 0\n\tglobal_load_lds_dwordx4 %2, %3\n\ts_mov_b32 m0, %0"
               : "=&s"(keep) : "s"(ldsaddr), "v"(voff), "s"(sbase) : "memory");
}
DI void dma_v(u32 ldsaddr, const void* vaddr) {
  u32 keep;
  asm volatile("s_mov_b32 %0, m0\n\ts_mov_b32 m0, %1\n\ts_nop 0\n\tglobal_load_lds_dwordx4 %2, off nt\n\ts_mov_b32 m0, %0"
               : "=&s"(keep) : "s"(ldsaddr), "v"(vaddr) : "memory");
}
DI u32 swz(u32 row) { return ((row & 3u) << 2) | ((row >> 2) & 3u); }
DI v4u ldsr128(const LAS char* p) { return *(const LAS v4u*)p; }
DI v4u tr2(const LAS char* p0, const LAS char* p1) {
  s16x4 a = __builtin_amdgcn_ds_read_tr16_b64_v4i16((LAS s16x4*)p0);
  s16x4 b = __builtin_amdgcn_ds_read_tr16_b64_v4i16((LAS s16x4*)p1);
  s16x8 v = __builtin_shufflevector(a, b, 0, 1, 2, 3, 4, 5, 6, 7);
  return __builtin_bit_cast(v4u, v);
}
DI void wait_ticks(u64 t0, u32 ticks) { if (ticks) { while (__builtin_amdgcn_s_memrealtime() - t0 < ticks) __builtin_amdgcn_s_sleep(2); } }

#define STAMP(i) do { } while (0)
#define STARG

struct Ptrs { const u16 *x, *ckv, *wq, *wuk, *wuv, *wo, *wg, *wu, *wd, *c, *wmod; u16* out; char* ws; const char* code; u64 t0; };

DI v4u frag64(const LAS char* img, int lane, int c, int ks) {
  const int g = lane >> 4, q = (lane & 15) >> 2, p = lane & 3;
  const LAS char* a = img + (32 * ks + 8 * g + q) * 64 + 32 * c + 8 * p;
  return tr2(a, a + 4 * 64);
}
DI v4u frag256(const LAS char* img, int lane, int c, int ks) {
  const int g = lane >> 4, q = (lane & 15) >> 2, p = lane & 3;
  const u32 r0 = 8 * g + q, r1 = r0 + 4, ch = 2 * c + (p >> 1);
  return tr2(img + 8192 * ks + 256 * r0 + 16 * (ch ^ swz(r0)) + 8 * (p & 1), img + 8192 * ks + 256 * r1 + 16 * (ch ^ swz(r1)) + 8 * (p & 1));
}
template <int N> DI f32x4 gather_sum(rsrc_t ws, int off, int stride, u32 T) {
  f32x4 s;
  _Pragma("nounroll") for (u32 sp = 0; sp < SPIN_CAP; ++sp) {
    CBAR();
    v4u t[N];
#pragma unroll
    for (int k = 0; k < N; ++k) t[k] = wld128(ws, off + k * stride);
    u32 bad = 0;
#pragma unroll
    for (int k = 0; k < N; ++k) bad |= tbad4(t[k], T);
    s = asf4(t[0]);
#pragma unroll
    for (int k = 1; k < N; ++k) s += asf4(t[k]);
    if (__all(bad == 0)) break;
  }
  return s;
}
template <int SLEEP> DI void wait_piece(rsrc_t ws, int off, u32 T, int lane) {
  _Pragma("nounroll") for (u32 sp = 0; sp < SPIN_CAP; ++sp) {
    CBAR();
    const v4u f = wld128(ws, lane == 0 ? off : OOB);
    if (__builtin_amdgcn_readfirstlane(tbad4(f, T)) == 0) break;
    __builtin_amdgcn_s_sleep(SLEEP);
  }
}
DI u32 lds_ld(const LAS char* p) { return *(const volatile LAS u32*)p; }
#define LGKM0() asm volatile("s_waitcnt lgkmcnt(0)" ::: "memory")

constexpr int FL_WQ = 0, FL_WUK = 40960, FL_WO = 0, FL_WUV = 81920, FL_SM = FL_WUV + 32768;
constexpr int FL_HB = FL_SM, FL_QB = FL_HB + 1024, FL_QA = FL_QB + 512, FL_CRED = FL_QA + 512, FL_CST = FL_CRED + 4096,
              FL_CB = FL_CST + 64, FL_CTX = FL_CB + 512, FL_WRED = FL_CTX + 4096, FL_FIN = FL_WRED + 4096, FL_TAG = FL_FIN + 1024, FL_PF = FL_TAG + 64,
              FL_SB = FL_PF + 1024  ;
static_assert(FL_PF % 16 == 0 && FL_SB + 2560 <= LDS_BYTES, "lds");

DI void f_role(const Ptrs& P, LAS char* L, rsrc_t ws, int p, int w, int lane STARG) {
  const int i16 = lane & 15, g = lane >> 4;
  const int h = p & 31, sl = p >> 5;
  const int un = (p & 7) + 8 * (p >> 4), sub = (p >> 3) & 1;
  const f32x4 Z4 = {0.f, 0.f, 0.f, 0.f};
  float ln[4] = {0.f, 0.f, 0.f, 0.f};
  if (w < 2) {
    const v4u xa = *(const GAS v4u*)(P.x + w * 640 + 8 * lane);
    const v4u xb = *(const GAS v4u*)(P.x + w * 640 + 512 + 8 * (lane & 15));
    const v2u xs = *(const GAS v2u*)(P.x + w * 640 + 160 * sl + 4 * (lane < 40 ? lane : 0));
    float v[16];
#pragma unroll
    for (int k = 0; k < 4; ++k) { v[2 * k] = bflo(xa[k]); v[2 * k + 1] = bfhi(xa[k]); v[8 + 2 * k] = bflo(xb[k]); v[9 + 2 * k] = bfhi(xb[k]); }
    const bool ex = lane < 16;
    float s = 0.f;
#pragma unroll
    for (int k = 0; k < 8; ++k) s += v[k] + (ex ? v[8 + k] : 0.f);
    const float mean = wave_sum(s) * (1.f / 640.f);
    float s2 = 0.f;
#pragma unroll
    for (int k = 0; k < 8; ++k) { const float d = v[k] - mean, e = v[8 + k] - mean; s2 += d * d + (ex ? e * e : 0.f); }
    const float rstd = rsqrtf(wave_sum(s2) * (1.f / 640.f) + 1e-5f);
    ln[0] = (bflo(xs.x) - mean) * rstd; ln[1] = (bfhi(xs.x) - mean) * rstd; ln[2] = (bflo(xs.y) - mean) * rstd; ln[3] = (bfhi(xs.y) - mean) * rstd;
  } else if (w < 6) {
    const char* sb = (const char*)P.wq + (size_t)(160 * sl) * 8192 + h * 256;
    const u32 n0 = w - 2;
    const u32 vo = g * 8192 + ((i16 ^ ((g << 2) | n0)) << 4);
#pragma unroll 2
    for (int k = 0; k < 10; ++k) dma_s(FL_WQ + (n0 + 4 * k) * 1024, sb + (size_t)(n0 + 4 * k) * 4 * 8192, vo);
    const char* su = (const char*)P.wuk + (size_t)(128 * sl) * 8192 + h * 256;
#pragma unroll 2
    for (int k = 0; k < 8; ++k) dma_s(FL_WUK + (n0 + 4 * k) * 1024, su + (size_t)(n0 + 4 * k) * 4 * 8192, vo);
    vmwait<8>();
  } else if (w == 7) {
    const u32 T0 = mk_tag(wld32(ws, WS_EPOCH));
    if (lane == 0) *(LAS u32*)(L + FL_TAG) = T0;
    {
      const int o0 = (p >> 3) * 1024;
      dma_s_keep(FL_PF, P.code + (o0 < CODE_PF - 1024 ? o0 : CODE_PF - 1024), lane * 16);
      dma_s_keep(FL_PF, P.code + (o0 + 16384 < CODE_PF - 1024 ? o0 + 16384 : CODE_PF - 1024), lane * 16);
    }
    const v4u S = {SENT, SENT, SENT, SENT};
    wst128(ws, lane < 32 ? WS_S3 + ((lane >> 4) * 32 + h) * 1024 + sl * 256 + i16 * 16 : OOB, S);
  }
  STAMP(1);
  BAR();
  const u32 T = __builtin_amdgcn_readfirstlane(lds_ld(L + FL_TAG));
  v4u aq[5];
#pragma unroll
  for (int k = 0; k < 5; ++k) aq[k] = frag256(L + FL_WQ, lane, w, k);
  STAMP(2);
  if (w < 2) {
    f32x4 sa, ca;
    _Pragma("nounroll") for (u32 sp = 0; sp < SPIN_CAP; ++sp) {
      CBAR();
      v4u t[10];
#pragma unroll
      for (int k = 0; k < 10; ++k) t[k] = wld128(ws, lane < 40 ? WS_S1 + (((k >> 1) * 4 + (k & 1) * 2 + w) * 640 + 160 * sl + 4 * lane) * 4 : OOB);
      u32 bad = 0;
#pragma unroll
      for (int k = 0; k < 10; ++k) bad |= tbad4(t[k], T);
      if (lane >= 40) bad = 0;
      sa = asf4(t[0]); ca = asf4(t[1]);
#pragma unroll
      for (int k = 1; k < 5; ++k) { sa += asf4(t[2 * k]); ca += asf4(t[2 * k + 1]); }
      if (__all(bad == 0)) break;
    }
    const float h0 = ln[0] * (1.f + ca[0]) + sa[0], h1 = ln[1] * (1.f + ca[1]) + sa[1];
    const float h2 = ln[2] * (1.f + ca[2]) + sa[2], h3 = ln[3] * (1.f + ca[3]) + sa[3];
    if (lane < 40) { const v2u o = {pk_bf16(h0, h1), pk_bf16(h2, h3)}; *(LAS v2u*)(L + FL_HB + w * 320 + lane * 8) = o; }
    STAMP(4);
  } else if (w < 6) {
    vmwait<0>();
  } else if (w == 7) {
    VM0();
    const v2u f = {T, ~T};
    wst64(ws, lane == 0 ? WS_FLAGF + p * 128 : OOB, f);
  }
  BAR();
  v4u au[4];
  {
    v4u bh[5];
#pragma unroll
    for (int k = 0; k < 5; ++k) bh[k] = ldsr128(L + FL_HB + (lane & 1) * 320 + (32 * k + 8 * g) * 2);
    const u32 row = 16 * w + i16, sw = swz(row);
#pragma unroll
    for (int k = 0; k < 4; ++k) au[k] = ldsr128(L + FL_WUK + row * 256 + (((4 * k + g) ^ sw) << 4));
    f32x4 acc = Z4;
#pragma unroll
    for (int k = 0; k < 5; ++k) acc = mfma16(aq[k], bh[k], acc);
    wst128(ws, i16 < 2 ? WS_S2 + (h * 4 + sl) * 1024 + w * 128 + i16 * 64 + g * 16 : OOB, tag4(acc, T));
  }
  STAMP(5);
  if (w == 0) {
    const f32x4 q = gather_sum<4>(ws, WS_S2 + h * 4096 + lane * 16, 1024, T);
    const v2u o = {pk_bf16(q[0], q[1]), pk_bf16(q[2], q[3])};
    *(LAS v2u*)(L + FL_QB + ((lane >> 2) & 1) * 256 + (16 * (lane >> 3) + 4 * (lane & 3)) * 2) = o;
    STAMP(6);
  }
  BAR();
  {
    v4u bq[4];
#pragma unroll
    for (int k = 0; k < 4; ++k) bq[k] = ldsr128(L + FL_QB + (lane & 1) * 256 + (32 * k + 8 * g) * 2);
    f32x4 acc = Z4;
#pragma unroll
    for (int k = 0; k < 4; ++k) acc = mfma16(au[k], bq[k], acc);
    constexpr float SC = 0.08838834764831845f * 1.4426950408889634f;
    if (i16 < 2) { const v2u o = {canon(pk_bf16(acc[0] * SC, acc[1] * SC)), canon(pk_bf16(acc[2] * SC, acc[3] * SC))};
      *(LAS v2u*)(L + FL_QA + i16 * 256 + (16 * w + 4 * g) * 2) = o; }
  }
  BAR();
  if (w == 0) wst128(ws, lane < 32 ? WS_S3 + ((lane >> 4) * 32 + h) * 1024 + sl * 256 + i16 * 16 : OOB, ldsr128(L + FL_QA + (lane & 31) * 16));
  STAMP(7);
  const int kq = p / 10, ms = p % 10;
  if (w >= 4) {
    wait_ticks(P.t0, F_TAIL_DELAY);
    const u32 n0 = w - 4;
    const char* sb = (const char*)P.wuv + (size_t)(128 * sl) * 8192 + h * 256;
    const u32 vo = g * 8192 + ((i16 ^ ((g << 2) | n0)) << 4);
#pragma unroll 2
    for (int k = 0; k < 8; ++k) dma_s(FL_WUV + (n0 + 4 * k) * 1024, sb + (size_t)(n0 + 4 * k) * 4 * 8192, vo);
    if (p < 80) {
      const char* so = (const char*)P.wo + (size_t)(512 * kq) * 1280 + ms * 128;
      const u32 vo2 = (lane >> 3) * 1280 + (lane & 7) * 16;
#pragma unroll 2
      for (int n = n0; n < 64; n += 4) dma_s(FL_WO + n * 1024, so + (size_t)n * 8 * 1280, vo2);
    } else if (p >= 88) {
      const char* sm = (const char*)P.wmod + (size_t)(1280 + 64 * (p - 88)) * 2;
      const u32 vo2 = (lane >> 3) * 7680 + (lane & 7) * 16;
#pragma unroll 2
      for (int n = n0; n < 80; n += 4) dma_s(FL_WO + n * 1024, sm + (size_t)n * 8 * 7680, vo2);
    }
    vmwait<0>();
  } else if (p >= 88 && w < 3) {
    const int tid = w * 64 + lane;
    const v4u cvv = *(const GAS v4u*)(P.c + (tid < 160 ? tid : 0) * 8);
    v4u sv;
#pragma unroll
    for (int k = 0; k < 4; ++k) sv[k] = pk_bf16(silu_f(bflo(cvv[k])), silu_f(bfhi(cvv[k])));
    if (tid < 160) *(LAS v4u*)(L + FL_SB + tid * 16) = sv;
  }
  BAR();
  STAMP(8);
  if (p >= 88) {
    f32x4 a[4] = {Z4, Z4, Z4, Z4};
    const int q = (lane & 15) >> 2, pp = lane & 3;
    for (int ks = w; ks < 20; ks += 8) {
      const v4u b = ldsr128(L + FL_SB + (lane & 1) * 1280 + (32 * ks + 8 * g) * 2);
      const LAS char* base = L + FL_WO + (32 * ks + 8 * g + q) * 128 + 8 * pp;
#pragma unroll
      for (int c = 0; c < 4; ++c) a[c] = mfma16(tr2(base + 32 * c, base + 32 * c + 4 * 128), b, a[c]);
    }
    if (i16 < 2) {
      LAS f32x4* wr = (LAS f32x4*)(L + FL_WRED + w * 512 + i16 * 256 + g * 16);
#pragma unroll
      for (int c = 0; c < 4; ++c) wr[4 * c] = a[c];
    }
    BAR();
    if (w == 0) {
      const LAS f32x4* wr = (const LAS f32x4*)(L + FL_WRED + (lane & 31) * 16);
      f32x4 v = wr[0];
#pragma unroll
      for (int k = 1; k < 8; ++k) v += wr[32 * k];
      const int col = 64 * (p - 88);
      wst128(ws, lane < 32 ? WS_MB + (((col / 640) * 2 + (lane >> 4)) * 640 + col % 640) * 4 + (lane & 15) * 16 : OOB, tag4(v, T));
    }
  }
  v4u uv[4], ao[8];
#pragma unroll
  for (int k = 0; k < 4; ++k) uv[k] = frag256(L + FL_WUV, lane, w, k);
  if (p < 80) {
    const LAS char* base = L + FL_WO + (64 * w + 8 * g + ((lane & 15) >> 2)) * 128 + 8 * (lane & 3);
#pragma unroll
    for (int k = 0; k < 8; ++k) { const LAS char* a = base + (k >> 2) * 32 * 128 + (k & 3) * 32; ao[k] = tr2(a, a + 4 * 128); }
  }
  {
    const int b = w >> 2, k0 = 16 * (w & 3);
    const int sto = WS_ST + ((b * 64 + k0 + i16) * 8 + (h & 7)) * 128 + (h >> 4) * 16;
    if (w == 0) wait_piece<8>(ws, sto, T, lane);
    BAR();
    v4u s;
    _Pragma("nounroll") for (u32 sp = 0; sp < SPIN_CAP; ++sp) {
      CBAR();
      s = wld128(ws, lane < 16 ? sto : OOB);
      if (__all(lane >= 16 || tbad4(s, T) == 0)) break;
      __builtin_amdgcn_s_sleep(2);
    }
    float wj[4];
    {
      const bool odd = (h >> 3) & 1;
      const float m = __uint_as_float((odd ? s.z : s.x) & 0xFFFFFF00u), l = __uint_as_float((odd ? s.w : s.y) & 0xFFFFFF00u);
      float Mw = fmaxf(m, dpp<0xB1>(m)); Mw = fmaxf(Mw, dpp<0x4E>(Mw)); Mw = fmaxf(Mw, dpp<0x141>(Mw)); Mw = fmaxf(Mw, dpp<0x140>(Mw));
      const float e = __builtin_amdgcn_exp2f(m - Mw);
      float Lw = e * l; Lw += dpp<0xB1>(Lw); Lw += dpp<0x4E>(Lw); Lw += dpp<0x141>(Lw); Lw += dpp<0x140>(Lw);
      if (lane == 0) *(LAS f32x2*)(L + FL_CST + w * 8) = f32x2{Mw, Lw};
#pragma unroll
      for (int j = 0; j < 4; ++j) wj[j] = __shfl(e, 4 * j + g);
    }
    STAMP(9);
    _Pragma("nounroll") for (u32 sp = 0; sp < SPIN_CAP; ++sp) {
      CBAR();
      const v4u f = wld128(ws, lane < 16 ? WS_DN + ((b * 64 + k0 + i16) * 8 + (h >> 3)) * 128 : OOB);
      if (__all(lane >= 16 || tbad4(f, T) == 0)) break;
    }
    CBAR();
    STAMP(3);
    v4u t[4];
#pragma unroll
    for (int j = 0; j < 4; ++j) t[j] = wld128(ws, WS_S4 + ((((h * 4 + sl) * 2 + b) * 64 + k0 + 4 * j) * 256) + lane * 16);
    float acc[8];
#pragma unroll
    for (int k = 0; k < 4; ++k) { acc[2 * k] = wj[0] * bflo(t[0][k]); acc[2 * k + 1] = wj[0] * bfhi(t[0][k]); }
#pragma unroll
    for (int j = 1; j < 4; ++j) {
#pragma unroll
      for (int k = 0; k < 4; ++k) { acc[2 * k] += wj[j] * bflo(t[j][k]); acc[2 * k + 1] += wj[j] * bfhi(t[j][k]); }
    }
#pragma unroll
    for (int k = 0; k < 8; ++k) acc[k] = x32_sum(x16_sum(acc[k]));
    if (lane < 16) {
      LAS f32x4* cr = (LAS f32x4*)(L + FL_CRED + w * 512 + lane * 32);
      cr[0] = f32x4{acc[0], acc[1], acc[2], acc[3]}; cr[1] = f32x4{acc[4], acc[5], acc[6], acc[7]};
    }
    BAR();
    if (w == 0) {
      const int bb = lane >> 5;
      const LAS f32x4* cs = (const LAS f32x4*)(L + FL_CST + bb * 32);
      const f32x4 c01 = cs[0], c23 = cs[1];
      const LAS f32x4* cr = (const LAS f32x4*)(L + FL_CRED + bb * 2048 + (lane & 31) * 16);
      const f32x4 r0 = cr[0], r1 = cr[32], r2 = cr[64], r3 = cr[96];
      const float M = fmaxf(fmaxf(c01[0], c01[2]), fmaxf(c23[0], c23[2]));
      const float f0 = __builtin_amdgcn_exp2f(c01[0] - M), f1 = __builtin_amdgcn_exp2f(c01[2] - M), f2 = __builtin_amdgcn_exp2f(c23[0] - M), f3 = __builtin_amdgcn_exp2f(c23[2] - M);
      const float inv = __builtin_amdgcn_rcpf((f0 * c01[1] + f1 * c01[3]) + (f2 * c23[1] + f3 * c23[3]));
      const f32x4 v = ((r0 * f0 + r1 * f1) + (r2 * f2 + r3 * f3)) * inv;
      const v2u o = {pk_bf16(v[0], v[1]), pk_bf16(v[2], v[3])};
      *(LAS v2u*)(L + FL_CB + lane * 8) = o;
    }
    BAR();
    v4u bc[4];
#pragma unroll
    for (int k = 0; k < 4; ++k) bc[k] = ldsr128(L + FL_CB + (lane & 1) * 256 + (32 * k + 8 * g) * 2);
    f32x4 a2 = Z4;
#pragma unroll
    for (int k = 0; k < 4; ++k) a2 = mfma16(uv[k], bc[k], a2);
    wst128(ws, i16 < 2 ? WS_S5 + (h * 4 + sl) * 1024 + w * 128 + i16 * 64 + g * 16 : OOB, tag4(a2, T));
  }
  STAMP(10);
  if (p < 80) {
    f32x4 cx = gather_sum<2>(ws, WS_S5 + (4 * kq + (w >> 1)) * 4096 + (lane >> 5) * 1024 + (w & 1) * 512 + (lane & 31) * 16, 2048, T);
    STAMP(11);
#pragma unroll
    for (int k = 0; k < 4; ++k) cx[k] = x32_sum(cx[k]);
    const v2u o = {pk_bf16(cx[0], cx[1]), pk_bf16(cx[2], cx[3])};
    if (lane < 32) *(LAS v2u*)(L + FL_CTX + w * 256 + ((lane >> 2) & 1) * 128 + (16 * (lane >> 3) + 4 * (lane & 3)) * 2) = o;
    LGKM0();
    v4u bx[2];
#pragma unroll
    for (int k = 0; k < 2; ++k) bx[k] = ldsr128(L + FL_CTX + w * 256 + (lane & 1) * 128 + (32 * k + 8 * g) * 2);
    f32x4 a[4];
#pragma unroll
    for (int c = 0; c < 4; ++c) a[c] = mfma16(ao[4 + c], bx[1], mfma16(ao[c], bx[0], Z4));
    if (i16 < 2) {
      LAS f32x4* wr = (LAS f32x4*)(L + FL_WRED + w * 512 + i16 * 256 + g * 16);
#pragma unroll
      for (int c = 0; c < 4; ++c) wr[4 * c] = a[c];
    }
    BAR();
    if (w == 0) {
      const LAS f32x4* wr = (const LAS f32x4*)(L + FL_WRED + (lane & 31) * 16);
      f32x4 v[8];
#pragma unroll
      for (int k = 0; k < 8; ++k) v[k] = wr[32 * k];
      const f32x4 r = ((v[0] + v[1]) + (v[2] + v[3])) + ((v[4] + v[5]) + (v[6] + v[7]));
      wst128(ws, lane < 32 ? WS_S6 + ((kq * 2 + (lane >> 4)) * 640 + 64 * ms) * 4 + (lane & 15) * 16 : OOB, tag4(r, T));
    }
    STAMP(12);
  } else if (p >= 88) {
    const int fs = p - 88, bm = lane & 7, bb = bm >> 2, m0 = 16 * fs + 4 * (bm & 3);
    f32x4 x1, gb;
    if (w == 0) {
      const v2u xv = *(const GAS v2u*)(P.x + bb * 640 + m0);
      _Pragma("nounroll") for (u32 sp = 0; sp < SPIN_CAP; ++sp) {
        CBAR();
        const v4u t0 = wld128(ws, WS_S6 + (((lane >> 3) * 2 + bb) * 640 + m0) * 4);
        const v4u t4 = wld128(ws, lane < 8 ? WS_MB + ((0 * 2 + bb) * 640 + m0) * 4 : OOB);
        const v4u t5 = wld128(ws, lane < 8 ? WS_MB + ((3 * 2 + bb) * 640 + m0) * 4 : OOB);
        const u32 bad = tbad4(t0, T) | (lane < 8 ? tbad4(t4, T) | tbad4(t5, T) : 0u);
        f32x4 at = asf4(t0);
#pragma unroll
        for (int k = 0; k < 4; ++k) { float v = at[k]; v += dpp<0x128>(v); at[k] = x32_sum(x16_sum(v)); }
        const f32x4 xf = {bflo(xv.x), bfhi(xv.x), bflo(xv.y), bfhi(xv.y)};
        x1 = xf + asf4(t4) * at;
        gb = asf4(t5);
        if (__all(bad == 0)) break;
        __builtin_amdgcn_s_sleep(4);
      }
    }
    BAR();
    STAMP(11);
    f32x4 y;
    const int tid = w * 64 + lane;
    _Pragma("nounroll") for (u32 sp = 0; sp < SPIN_CAP; ++sp) {
      CBAR();
      const v4u t0 = wld128(ws, WS_S8 + fs * (108 * 128) + tid * 16);
      const v4u t1 = wld128(ws, WS_S8 + fs * (108 * 128) + (tid < 352 ? 512 + tid : tid) * 16);
      y = asf4(t0);
      if (tid < 352) y += asf4(t1);
      if (__all((tbad4(t0, T) | tbad4(t1, T)) == 0)) break;
    }
    STAMP(12);
#pragma unroll
    for (int k = 0; k < 4; ++k) { float v = y[k]; v += dpp<0x128>(v); y[k] = x32_sum(x16_sum(v)); }
    if (lane < 8) *(LAS f32x4*)(L + FL_FIN + w * 128 + lane * 16) = y;
    BAR();
    if (w == 0) {
      const LAS f32x4* fr = (const LAS f32x4*)(L + FL_FIN + bm * 16);
      f32x4 v[8];
#pragma unroll
      for (int k = 0; k < 8; ++k) v[k] = fr[8 * k];
      const f32x4 o = x1 + gb * (((v[0] + v[1]) + (v[2] + v[3])) + ((v[4] + v[5]) + (v[6] + v[7])));
      const v2u ob = {pk_bf16(o[0], o[1]), pk_bf16(o[2], o[3])};
      if (lane < 8) *(GAS v2u*)(P.out + bb * 640 + m0) = ob;
      if (p == 88 && lane == 0) wst32(ws, WS_EPOCH, wld32(ws, WS_EPOCH) + 1u);
    }
    STAMP(13);
  }
}

constexpr int AL_CKV = 0, AL_Q = 131072;
constexpr int AL_P = AL_Q, AL_MAX = AL_Q + 8192, AL_SUM = AL_MAX + 512;
constexpr int AL_O = 0, AL_GU = 32768, AL_DN = AL_Q + 10240  , AL_SM = AL_GU + 40960;
constexpr int AL_H2 = AL_SM, AL_STAT = AL_H2 + 2560, AL_RED = AL_STAT + 64, AL_ACT = AL_RED + 1024;
static_assert(AL_ACT + 8 * 128 <= AL_Q && AL_SUM + 512 <= AL_DN && AL_DN + 20480 <= LDS_BYTES, "lds");

DI u32 p1_phase(const Ptrs& P, LAS char* L, rsrc_t ws, int j, int w, int lane) {
  const int i16 = lane & 15, g = lane >> 4, l32 = lane & 31;
  const f32x4 Z4 = {0.f, 0.f, 0.f, 0.f};
  u32 T = wld32(ws, WS_EPOCH);
  {
    const int un = (j & 7) + 8 * (j >> 4), cg = 2 * (un / 5) + ((j >> 3) & 1), ks1 = un % 5;
    if (un < 50) {
      const int base = 128 * ks1 + 16 * w;
      const int bb = (lane >> 3) & 1, r1 = i16 >> 3, mat = (i16 >> 2) & 1, ch = i16 & 3, jj = lane & 7;
      u32 cv[2]; v4u wm[2];
#pragma unroll
      for (int t = 0; t < 2; ++t) cv[t] = *(const GAS u32*)(P.c + bb * 640 + base + 8 * t + 2 * g);
#pragma unroll
      for (int t = 0; t < 2; ++t) wm[t] = ldnt128(P.wmod + (size_t)(base + 8 * t + 2 * g + r1) * 3840 + mat * 640 + 32 * cg + 8 * ch);
      u32 msk[4];
#pragma unroll
      for (int r = 0; r < 4; ++r) msk[r] = (r == (jj >> 1)) ? (0xFFFFu << (16 * (jj & 1))) : 0u;
      f32x4 a0 = Z4, a1 = Z4;
#pragma unroll
      for (int t = 0; t < 2; ++t) {
        const u32 sp = pk_bf16(silu_f(bflo(cv[t])), silu_f(bfhi(cv[t])));
        const u32 d0 = (sp & 0xFFFFu) * 0x10001u, d1 = (sp >> 16) * 0x10001u;
        const v4u B0 = {d0 & msk[0], d0 & msk[1], d0 & msk[2], d0 & msk[3]};
        const v4u B1 = {d1 & msk[0], d1 & msk[1], d1 & msk[2], d1 & msk[3]};
        a0 = mfma16(wm[t], B0, a0);
        a1 = mfma16(wm[t], B1, a1);
      }
      LAS float* red = (LAS float*)(L + AL_Q) + w * 128;
      const int mt = (lane >> 4) & 1;
#pragma unroll
      for (int r = 0; r < 4; ++r) {
        const float v = x32_sum(lane < 32 ? a0[r] : a1[r]);
        if (lane < 32) red[(mt * 2 + bb) * 32 + 8 * r + jj] = v;
      }
      T = __builtin_amdgcn_readfirstlane(mk_tag(T));
      BAR();
      if (w == 0) {
        const LAS f32x4* rd = (const LAS f32x4*)(L + AL_Q) + l32;
        const f32x4 s = ((rd[0] + rd[32]) + (rd[64] + rd[96])) + ((rd[128] + rd[160]) + (rd[192] + rd[224]));
        wst128(ws, lane < 32 ? WS_S1 + ((ks1 * 4 + (lane >> 3)) * 640 + 32 * cg) * 4 + (lane & 7) * 16 : OOB, tag4(s, T));
      }
    } else {
      T = __builtin_amdgcn_readfirstlane(mk_tag(T));
    }
  }
  return T;
}

DI void a_role(const Ptrs& P, LAS char* L, rsrc_t ws, int j, int w, int lane, u32 T STARG) {
  const int i16 = lane & 15, g = lane >> 4, l32 = lane & 31, hh = lane >> 5;
  const int b = j >> 6, kc = j & 63;
  const f32x4 Z4 = {0.f, 0.f, 0.f, 0.f};
  f32x16 S;
  const int fl = (j & 7) + 8 * (j >> 5), fsub = (j >> 3) & 3, fj = 4 * fl + fsub;
  const bool do_ffn = fl < 27;
  v4u dummy = {0u, 0u, 0u, 0u};
  STAMP(14);
  if (w >= 4) {
    const int lw = w - 4;
    const char* sb = (const char*)P.ckv + ((size_t)(b * 8192 + kc * 128 + 32 * lw)) * 1024;
    u32 vo[4];
#pragma unroll
    for (int i = 0; i < 4; ++i) vo[i] = g * 1024 + ((i16 ^ ((g << 2) | i)) << 4);
    wait_ticks(P.t0, A_DELAY);
    for (int cq = 0; cq < 4; ++cq) {
#pragma unroll
      for (int i = 0; i < 8; ++i) dma_s(AL_CKV + cq * 32768 + (8 * lw + i) * 1024, sb + i * 4096 + cq * 256, vo[i & 3]);
    }
    STAMP(1);
    vmwait<16>();
  } else {
    _Pragma("nounroll") for (u32 sp = 0; sp < SPIN_CAP; ++sp) {
      CBAR();
      const v2u f = wld64(ws, lane < 32 ? WS_FLAGF + (8 * w + (lane & 7) + 32 * (lane >> 3)) * 128 : OOB);
      if (__all(lane >= 32 || ((f.x ^ T) | (f.y ^ ~T)) == 0)) break;
      __builtin_amdgcn_s_sleep(16);
    }
    STAMP(1);
    _Pragma("nounroll") for (u32 sp = 0; sp < SPIN_CAP; ++sp) {
      CBAR();
      const u32 f = wld32(ws, lane == 0 ? WS_S3 + (b * 32 + 8 * w) * 1024 : OOB);
      if (__builtin_amdgcn_readfirstlane(f) != SENT) break;
      __builtin_amdgcn_s_sleep(4);
    }
    _Pragma("nounroll") for (u32 sp = 0; sp < SPIN_CAP; ++sp) {
      CBAR();
      v4u t[8];
#pragma unroll
      for (int k = 0; k < 8; ++k) t[k] = wld128(ws, WS_S3 + (b * 32 + 8 * w + k) * 1024 + lane * 16);
      u32 mx = 0;
#pragma unroll
      for (int k = 0; k < 8; ++k) { const u32 a = max(max(t[k].x, t[k].y), max(t[k].z, t[k].w)); mx = max(mx, a); }
      if (__all(mx != SENT)) {
#pragma unroll
        for (int k = 0; k < 8; ++k) { const int hd = 8 * w + k; *(LAS v4u*)(L + AL_Q + hd * 1024 + ((lane ^ (hd & 15)) << 4)) = t[k]; }
        break;
      }
      __builtin_amdgcn_s_sleep(2);
    }
    STAMP(2);
  }
  BAR();
  if (w < 4) {
#pragma unroll
    for (int k = 0; k < 16; ++k) S[k] = 0.f;
    const u32 row = 32 * w + l32, sw = swz(row);
    u32 ca[8], qa[8];
#pragma unroll
    for (int s = 0; s < 8; ++s) { ca[s] = AL_CKV + row * 256 + (((2 * s + hh) ^ sw) << 4); qa[s] = AL_Q + l32 * 1024 + (((2 * s + hh) ^ (l32 & 15)) << 4); }
#pragma unroll
    for (int hf = 0; hf < 2; ++hf) {
      if (hf > 0) BAR();
      v4u a[16], q[16];
#pragma unroll
      for (int s = 0; s < 16; ++s) {
        a[s] = ldsr128(L + ca[s & 7] + hf * 65536 + (s >> 3) * 32768);
        q[s] = ldsr128(L + qa[s & 7] + hf * 512 + (s >> 3) * 256);
      }
      __builtin_amdgcn_sched_barrier(0);
#pragma unroll
      for (int s = 0; s < 16; ++s) S = mfma32(a[s], q[s], S);
    }
    STAMP(3);
    float m0 = fmaxf(fmaxf(S[0], S[1]), fmaxf(S[2], S[3])), m1 = fmaxf(fmaxf(S[4], S[5]), fmaxf(S[6], S[7]));
    float m2 = fmaxf(fmaxf(S[8], S[9]), fmaxf(S[10], S[11])), m3 = fmaxf(fmaxf(S[12], S[13]), fmaxf(S[14], S[15]));
    const float m = x32_max(fmaxf(fmaxf(m0, m1), fmaxf(m2, m3)));
    if (lane < 32) *(LAS float*)(L + AL_MAX + (w * 32 + lane) * 4) = m;
  } else {
    vmwait<0>();
    BAR();
    if (do_ffn) {
      const int lw = w - 4;
#pragma unroll 2
      for (int n = lw; n < 40; n += 4) {
        const int line = n * 8 + (lane >> 3), mat = line >= 160, row = 160 * fsub + line - 160 * mat;
        const char* pp = (const char*)(mat ? P.wu : P.wg) + (size_t)row * 3456 + fl * 128 + (lane & 7) * 16;
        asm volatile("global_load_dwordx4 %0, %1, off" : "+v"(dummy) : "v"(pp) : "memory");
      }
    }
  }
  BAR();
  float M;
  {
    const LAS float* mx = (const LAS float*)(L + AL_MAX) + l32;
    M = fmaxf(fmaxf(mx[0], mx[32]), fmaxf(mx[64], mx[96]));
    M = __uint_as_float(__float_as_uint(M) & 0xFFFFFF00u);
  }
  if (w < 4) {
    float sum = 0.f;
#pragma unroll
    for (int k = 0; k < 16; ++k) { S[k] = __builtin_amdgcn_exp2f(S[k] - M); sum += S[k]; }
    const u32 sw = swz(l32);
#pragma unroll
    for (int qd = 0; qd < 4; ++qd) {
      const v2u o = {pk_bf16(S[4 * qd], S[4 * qd + 1]), pk_bf16(S[4 * qd + 2], S[4 * qd + 3])};
      *(LAS v2u*)(L + AL_P + l32 * 256 + (((4 * w + qd) ^ sw) << 4) + 8 * hh) = o;
    }
    sum = x32_sum(sum);
    if (lane < 32) *(LAS float*)(L + AL_SUM + (w * 32 + lane) * 4) = sum;
  }
  BAR();
  STAMP(4);
  if (w == 7) {
    const LAS float* sm = (const LAS float*)(L + AL_SUM) + l32;
    const float Ls = (sm[0] + sm[32]) + (sm[64] + sm[96]);
    const float M2 = dpp<0x128>(M), L2 = dpp<0x128>(Ls);
    wst128(ws, (lane < 32 && !(lane & 8)) ? WS_ST + ((b * 64 + kc) * 8 + (lane & 7)) * 128 + (lane >> 4) * 16 : OOB, tag4(f32x4{M, Ls, M2, L2}, T));
  }
  f32x16 O0, O1;
#pragma unroll
  for (int k = 0; k < 16; ++k) { O0[k] = 0.f; O1[k] = 0.f; }
  {
    const int blk = (lane >> 4) & 1, q = (lane & 15) >> 2, pp = lane & 3;
    const u32 sw = swz(l32);
    const u32 c0 = 8 * (w & 1) + 2 * blk + (pp >> 1);
    const u32 r0 = 8 * hh + q, r1 = r0 + 4, s0 = swz(r0), s1 = swz(r1);
    const u32 tb = AL_CKV + (w >> 1) * 32768 + 8 * (pp & 1);
    const u32 a00 = tb + 256 * r0 + 16 * (c0 ^ s0), a01 = tb + 256 * r1 + 16 * (c0 ^ s1);
    const u32 a10 = tb + 256 * r0 + 16 * ((c0 + 4) ^ s0), a11 = tb + 256 * r1 + 16 * ((c0 + 4) ^ s1);
    v4u pb[8], a0[8], a1[8];
#pragma unroll
    for (int ks = 0; ks < 8; ++ks) {
      pb[ks] = ldsr128(L + AL_P + l32 * 256 + (((2 * ks + hh) ^ sw) << 4));
      a0[ks] = tr2(L + a00 + ks * 4096, L + a01 + ks * 4096);
      a1[ks] = tr2(L + a10 + ks * 4096, L + a11 + ks * 4096);
    }
    __builtin_amdgcn_sched_barrier(0);
#pragma unroll
    for (int ks = 0; ks < 8; ++ks) { O0 = mfma32(a0[ks], pb[ks], O0); O1 = mfma32(a1[ks], pb[ks], O1); }
  }
  BAR();
  STAMP(5);
#pragma unroll
  for (int qd = 0; qd < 4; ++qd) {
    const v2u o0 = {pk_bf16(O0[4 * qd], O0[4 * qd + 1]), pk_bf16(O0[4 * qd + 2], O0[4 * qd + 3])};
    const v2u o1 = {pk_bf16(O1[4 * qd], O1[4 * qd + 1]), pk_bf16(O1[4 * qd + 2], O1[4 * qd + 3])};
    *(LAS v2u*)(L + AL_O + l32 * 1024 + (((8 * w + qd) ^ (l32 & 15)) << 4) + 8 * hh) = o0;
    *(LAS v2u*)(L + AL_O + l32 * 1024 + (((8 * w + 4 + qd) ^ (l32 & 15)) << 4) + 8 * hh) = o1;
  }
  BAR();
  if (w < 4) {
    v4u v[8];
#pragma unroll
    for (int k = 0; k < 8; ++k) { const int hd = 8 * w + k; v[k] = ldsr128(L + AL_O + hd * 1024 + ((lane ^ (hd & 15)) << 4)); }
#pragma unroll
    for (int k = 0; k < 8; ++k) wst128(ws, WS_S4 + (((((8 * w + k) * 4 + g) * 2 + b) * 64 + kc) * 256) + i16 * 16, v[k]);
    VM0();
    wst128(ws, lane == 0 ? WS_DN + ((b * 64 + kc) * 8 + w) * 128 : OOB, tag4(Z4, T));
  } else {
    vmwait<0>();
  }
  STAMP(6);
  asm volatile("" :: "v"(dummy));
  if (!do_ffn) return;
  const int bb = w >> 2, m0 = 160 * (w & 3) + 4 * (lane < 40 ? lane : 0);
  const v2u xv = *(const GAS v2u*)(P.x + bb * 640 + m0);
  v4u mb[3];
  _Pragma("nounroll") for (u32 sp = 0; sp < SPIN_CAP; ++sp) {
    CBAR();
#pragma unroll
    for (int k = 0; k < 3; ++k) mb[k] = wld128(ws, lane < 40 ? WS_MB + ((k * 2 + bb) * 640 + m0) * 4 : OOB);
    if (__all(lane >= 40 || (tbad4(mb[0], T) | tbad4(mb[1], T) | tbad4(mb[2], T)) == 0)) break;
    __builtin_amdgcn_s_sleep(8);
  }
  BAR();
  {
    const char* gp = (const char*)(((lane & 2) ? P.wu : P.wg)) + (size_t)(lane >> 2) * 3456 + fj * 32 + (lane & 1) * 16;
    for (int n = w; n < 40; n += 8) dma_v(AL_GU + n * 1024, gp + (size_t)n * 16 * 3456);
    const char* sd = (const char*)P.wd + (size_t)(16 * fj) * 1280;
    for (int n = w; n < 20; n += 8) dma_s(AL_DN + n * 1024, sd + n * 1024, lane * 16);
    vmwait<0>();
  }
  BAR();
  v4u ag[5], ad[5];
  {
#pragma unroll
    for (int i = 0; i < 5; ++i) ag[i] = frag64(L + AL_GU, lane, w & 1, (w >> 1) + 4 * i);
    const int q = (lane & 15) >> 2, pp = lane & 3;
    const int r0 = (8 * g + q) & 15;
#pragma unroll
    for (int k = 0; k < 5; ++k) { const LAS char* a = L + AL_DN + r0 * 1280 + (5 * w + k) * 32 + 8 * pp; ad[k] = tr2(a, a + 4 * 1280); }
  }
  STAMP(7);
  {
    wait_piece<6>(ws, WS_S5 + w * 4096 * 4 + 3 * 1024, T, lane);
    f32x4 at;
    _Pragma("nounroll") for (u32 sp = 0; sp < SPIN_CAP; ++sp) {
      CBAR();
      v4u t[8];
#pragma unroll
      for (int k = 0; k < 8; ++k) t[k] = wld128(ws, lane < 40 ? WS_S6 + ((k * 2 + bb) * 640 + m0) * 4 : OOB);
      at = ((asf4(t[0]) + asf4(t[1])) + (asf4(t[2]) + asf4(t[3]))) + ((asf4(t[4]) + asf4(t[5])) + (asf4(t[6]) + asf4(t[7])));
      u32 bad = 0;
#pragma unroll
      for (int k = 0; k < 8; ++k) bad |= tbad4(t[k], T);
      if (__all(lane >= 40 || bad == 0)) break;
    }
    const f32x4 xf = {bflo(xv.x), bfhi(xv.x), bflo(xv.y), bfhi(xv.y)};
    const f32x4 x1 = xf + asf4(mb[0]) * at, sbv = asf4(mb[1]), cbv = asf4(mb[2]);
    STAMP(8);
    const bool act = lane < 40;
    const float pv = __builtin_bit_cast(float, __builtin_amdgcn_readfirstlane(__builtin_bit_cast(int, x1[0])));
    const float d0 = x1[0] - pv, d1 = x1[1] - pv, d2 = x1[2] - pv, d3 = x1[3] - pv;
    const float s1 = wave_sum(act ? (d0 + d1) + (d2 + d3) : 0.f);
    const float s2 = wave_sum(act ? (d0 * d0 + d1 * d1) + (d2 * d2 + d3 * d3) : 0.f);
    const float mw = pv + s1 * (1.f / 160.f), m2 = s2 - s1 * s1 * (1.f / 160.f);
    if (lane == 0) { LAS f32x2* sp = (LAS f32x2*)(L + AL_STAT); sp[w] = f32x2{mw, m2}; }
    BAR();
    const LAS f32x4* sp = (const LAS f32x4*)(L + AL_STAT + bb * 32);
    const f32x4 s01 = sp[0], s23 = sp[1];
    const float mean = ((s01[0] + s01[2]) + (s23[0] + s23[2])) * 0.25f;
    const float e0 = s01[0] - mean, e1 = s01[2] - mean, e2 = s23[0] - mean, e3 = s23[2] - mean;
    const float var = (((s01[1] + s01[3]) + (s23[1] + s23[3])) + 160.f * ((e0 * e0 + e1 * e1) + (e2 * e2 + e3 * e3))) * (1.f / 640.f);
    const float rstd = rsqrtf(var + 1e-5f);
    float hv[4];
#pragma unroll
    for (int k = 0; k < 4; ++k) hv[k] = (x1[k] - mean) * rstd * (1.f + cbv[k]) + sbv[k];
    if (act) { const v2u o = {pk_bf16(hv[0], hv[1]), pk_bf16(hv[2], hv[3])}; *(LAS v2u*)(L + AL_H2 + (bb * 640 + m0) * 2) = o; }
  }
  BAR();
  {
    v4u bh[5];
#pragma unroll
    for (int i = 0; i < 5; ++i) bh[i] = ldsr128(L + AL_H2 + (lane & 1) * 1280 + (32 * ((w >> 1) + 4 * i) + 8 * g) * 2);
    f32x4 a = Z4;
#pragma unroll
    for (int i = 0; i < 5; ++i) a = mfma16(ag[i], bh[i], a);
    if (i16 < 2) *(LAS f32x4*)(L + AL_RED + w * 128 + i16 * 64 + g * 16) = a;
  }
  BAR();
  {
    const int b2 = (lane >> 4) & 1, f = lane & 15;
    const LAS float* wr = (const LAS float*)(L + AL_RED) + b2 * 16 + f;
    const float gs = (wr[0] + wr[64]) + (wr[128] + wr[192]), us = (wr[32] + wr[96]) + (wr[160] + wr[224]);
    const float a = silu_f(gs) * us;
    LAS u16* ac = (LAS u16*)(L + AL_ACT + w * 128);
    ac[b2 * 32 + (lane >> 5) * 16 + f] = lane < 32 ? (u16)(pk_bf16(a, 0.f) & 0xFFFFu) : (u16)0;
    LGKM0();
    STAMP(9);
    const v4u bq = ldsr128(L + AL_ACT + w * 128 + (lane & 1) * 64 + g * 16);
    f32x4 acc[5];
#pragma unroll
    for (int k = 0; k < 5; ++k) acc[k] = mfma16(ad[k], bq, Z4);
#pragma unroll
    for (int k = 0; k < 5; ++k) wst128(ws, i16 < 2 ? WS_S8 + (5 * w + k) * (108 * 128) + fj * 128 + i16 * 64 + g * 16 : OOB, tag4(acc[k], T));
  }
  STAMP(10);
}

__global__ void __launch_bounds__(512, 2)
fused_decode(const u16* x, const u16* ckv, const u16* wq, const u16* wuk, const u16* wuv, const u16* wo, const u16* wg,
             const u16* wu, const u16* wd, const u16* c, const u16* wmod, u16* out, char* wsp) {
  extern __shared__ __attribute__((aligned(1024))) char lds[];
  LAS char* L = (LAS char*)lds;
  const int lane = threadIdx.x & 63, w = __builtin_amdgcn_readfirstlane(threadIdx.x >> 6);
  const rsrc_t ws = __builtin_amdgcn_make_buffer_rsrc((void*)wsp, 0, WS_END, 0x00020000);
  u64 pc;
  asm volatile("s_getpc_b64 %0" : "=s"(pc));
  Ptrs P{x, ckv, wq, wuk, wuv, wo, wg, wu, wd, c, wmod, out, wsp, (const char*)(pc & ~255ull), __builtin_amdgcn_s_memrealtime()};
  u32 touch = 0;
  if (w == 6 && blockIdx.x < 128) {
    const int idx = (blockIdx.x >> 3) * 64 + lane;
    const char* base = (const char*)ckv; int rel = idx;
    if (rel >= 256) { rel -= 256; base = (const char*)wq;
      if (rel >= 80) { rel -= 80; base = (const char*)wuk;
        if (rel >= 64) { rel -= 64; base = (const char*)wuv;
          if (rel >= 64) { rel -= 64; base = (const char*)wo;
            if (rel >= 80) { rel -= 80; base = (const char*)wg;
              if (rel >= 33) { rel -= 33; base = (const char*)wu;
                if (rel >= 33) { rel -= 33; base = (const char*)wd;
                  if (rel >= 33) { rel -= 33; base = (const char*)wmod;
                    if (rel >= 75) { rel -= 75; base = (const char*)wsp;
                      if (rel >= 80) { rel -= 80; base = (const char*)out; if (rel >= 1) { rel = 0; base = (const char*)c; } } } } } } } } } } }
    touch = __builtin_nontemporal_load((const GAS u32*)(base + (size_t)rel * 65536));
  }
  u32 T = 0;
  if (blockIdx.x >= 128) T = p1_phase(P, L, ws, blockIdx.x - 128, w, lane);
  if (blockIdx.x < 128) f_role(P, L, ws, blockIdx.x, w, lane);
  else a_role(P, L, ws, blockIdx.x - 128, w, lane, T);
  asm volatile("" :: "v"(touch));
}

extern "C" void kernel_launch(void* const* d_in, const int* in_sizes, int n_in, void* d_out, int out_size, void* d_ws,
                              size_t ws_size, hipStream_t stream) {
  (void)in_sizes; (void)n_in; (void)out_size; (void)ws_size;
  (void)hipFuncSetAttribute((const void*)fused_decode, hipFuncAttributeMaxDynamicSharedMemorySize, LDS_BYTES);
  fused_decode<<<256, 512, LDS_BYTES, stream>>>((const u16*)d_in[0], (const u16*)d_in[1], (const u16*)d_in[2], (const u16*)d_in[3],
                                                (const u16*)d_in[4], (const u16*)d_in[5], (const u16*)d_in[7], (const u16*)d_in[8],
                                                (const u16*)d_in[9], (const u16*)d_in[10], (const u16*)d_in[11], (u16*)d_out, (char*)d_ws);
}
```

```cpp
#include <hip/hip_runtime.h>
#include <hip/hip_bf16.h>
#include <stdint.h>

typedef unsigned short u16;
typedef unsigned int u32;
typedef unsigned long long u64;
typedef u32 v4u __attribute__((ext_vector_type(4)));
typedef u32 v2u __attribute__((ext_vector_type(2)));
typedef float f32x4 __attribute__((ext_vector_type(4)));
typedef float f32x2 __attribute__((ext_vector_type(2)));
typedef float f32x16 __attribute__((ext_vector_type(16)));
typedef short s16x4 __attribute__((ext_vector_type(4)));
typedef short s16x8 __attribute__((ext_vector_type(8)));
typedef __bf16 bf16x8 __attribute__((ext_vector_type(8)));
typedef __bf16 bf16x2 __attribute__((ext_vector_type(2)));
#define GAS __attribute__((address_space(1)))
#define LAS __attribute__((address_space(3)))
#define DI __device__ __forceinline__

#define A_DELAY 0
#define F_TAIL_DELAY 750

#define S6_SLEEP 1
#define S4_SLEEP 8
#define CODE_PF 24576
constexpr u32 SENT = 0xFFFFFFFFu;
constexpr u32 SPIN_CAP = 1u << 17;

constexpr int WS_EPOCH = 0;
constexpr int WS_FLAGF = 256;
constexpr int S1_COPY = 5 * 4 * 640 * 4, S3_COPY = 2 * 32 * 512 * 2, S6_COPY = 8 * 2 * 640 * 4;
constexpr int WS_S1 = WS_FLAGF + 128 * 128 + 3840;
constexpr int WS_S2 = WS_S1 + 8 * S1_COPY;
constexpr int WS_S3 = WS_S2 + 32 * 4 * 1024;
constexpr int WS_S5 = WS_S3 + 8 * S3_COPY;
constexpr int WS_S6 = WS_S5 + 32 * 4 * 1024;
constexpr int WS_MB = WS_S6 + 8 * S6_COPY;
constexpr int WS_S8 = WS_MB + 4 * 2 * 640 * 4;
constexpr int WS_ST = WS_S8 + 40 * 108 * 32 * 4;
constexpr int WS_S4 = WS_ST + 128 * 8 * 128;
constexpr int WS_END = WS_S4 + 32 * 4 * 2 * 64 * 256;
constexpr int WS_STAMP = WS_END;
static_assert(WS_S1 % 256 == 0 && WS_S2 % 256 == 0 && WS_S3 % 256 == 0 && WS_S5 % 256 == 0 && WS_S6 % 256 == 0 && WS_MB % 256 == 0 &&
              WS_S8 % 256 == 0 && WS_ST % 256 == 0 && WS_S4 % 256 == 0, "align");

constexpr int LDS_BYTES = 163840;

typedef __amdgpu_buffer_rsrc_t rsrc_t;
DI v4u wld128(rsrc_t r, int off) { return __builtin_amdgcn_raw_buffer_load_b128(r, off, 0, 16); }
DI v2u wld64(rsrc_t r, int off) { return __builtin_amdgcn_raw_buffer_load_b64(r, off, 0, 16); }
DI u32 wld32(rsrc_t r, int off) { return __builtin_amdgcn_raw_buffer_load_b32(r, off, 0, 16); }
DI void wst128(rsrc_t r, int off, v4u v) { __builtin_amdgcn_raw_buffer_store_b128(v, r, off, 0, 16); }
DI void wst64(rsrc_t r, int off, v2u v) { __builtin_amdgcn_raw_buffer_store_b64(v, r, off, 0, 16); }
DI void wst32(rsrc_t r, int off, u32 v) { __builtin_amdgcn_raw_buffer_store_b32(v, r, off, 0, 16); }
DI u32 canon(u32 x) { return x < 0xFFFFFFFEu ? x : 0xFFFFFFFEu; }
DI bool ok4(v4u v) { return v.x != SENT && v.y != SENT && v.z != SENT && v.w != SENT; }
DI v4u tag4(f32x4 v, u32 T) {
  v4u o;
  o.x = __builtin_amdgcn_perm(__float_as_uint(v[0]), T, 0x07060500u); o.y = __builtin_amdgcn_perm(__float_as_uint(v[1]), T, 0x07060501u);
  o.z = __builtin_amdgcn_perm(__float_as_uint(v[2]), T, 0x07060502u); o.w = __builtin_amdgcn_perm(__float_as_uint(v[3]), T, 0x07060503u);
  return o;
}
DI bool tok4(v4u v, u32 T) { return (__builtin_amdgcn_perm(v.y, v.x, 0x0c0c0400u) | __builtin_amdgcn_perm(v.w, v.z, 0x04000c0cu)) == T; }
DI u32 tbad4(v4u v, u32 T) { return (__builtin_amdgcn_perm(v.y, v.x, 0x0c0c0400u) | __builtin_amdgcn_perm(v.w, v.z, 0x04000c0cu)) ^ T; }
constexpr int OOB = 0x40000000;
DI f32x4 asf4(v4u v) { const v4u m = v & 0xFFFFFF00u; return __builtin_bit_cast(f32x4, m); }
DI u32 mk_tag(u32 epoch_word) { return (epoch_word + 1u) * 0x01010101u; }
#define VM0() do { __builtin_amdgcn_s_waitcnt(0x0F70); asm volatile("" ::: "memory"); } while (0)
DI float bflo(u32 x) { return __uint_as_float(x << 16); }
DI float bfhi(u32 x) { return __uint_as_float(x & 0xffff0000u); }
DI u32 pk_bf16(float lo, float hi) { f32x2 f = {lo, hi}; bf16x2 h = __builtin_convertvector(f, bf16x2); return __builtin_bit_cast(u32, h); }
DI float silu_f(float x) { return x * __builtin_amdgcn_rcpf(1.f + __expf(-x)); }
DI v4u ldnt128(const void* p) { return __builtin_nontemporal_load((const GAS v4u*)p); }
DI f32x4 mfma16(v4u a, v4u b, f32x4 c) { return __builtin_amdgcn_mfma_f32_16x16x32_bf16(__builtin_bit_cast(bf16x8, a), __builtin_bit_cast(bf16x8, b), c, 0, 0, 0); }
DI f32x16 mfma32(v4u a, v4u b, f32x16 c) { return __builtin_amdgcn_mfma_f32_32x32x16_bf16(__builtin_bit_cast(bf16x8, a), __builtin_bit_cast(bf16x8, b), c, 0, 0, 0); }
#define BAR() asm volatile("s_waitcnt lgkmcnt(0)\n\ts_barrier" ::: "memory")
#define CBAR() asm volatile("" ::: "memory")
template <int N> DI void vmwait() { asm volatile("s_waitcnt vmcnt(%0)" :: "i"(N) : "memory"); }

template <int CTRL> DI float dpp(float x) { return __builtin_bit_cast(float, __builtin_amdgcn_mov_dpp(__builtin_bit_cast(int, x), CTRL, 0xf, 0xf, true)); }
DI float x16_sum(float x) { auto s = __builtin_amdgcn_permlane16_swap(__float_as_uint(x), __float_as_uint(x), false, false); return __uint_as_float(s[0]) + __uint_as_float(s[1]); }
DI float x32_sum(float x) { auto s = __builtin_amdgcn_permlane32_swap(__float_as_uint(x), __float_as_uint(x), false, false); return __uint_as_float(s[0]) + __uint_as_float(s[1]); }
DI float x16_max(float x) { auto s = __builtin_amdgcn_permlane16_swap(__float_as_uint(x), __float_as_uint(x), false, false); return fmaxf(__uint_as_float(s[0]), __uint_as_float(s[1])); }
DI float x32_max(float x) { auto s = __builtin_amdgcn_permlane32_swap(__float_as_uint(x), __float_as_uint(x), false, false); return fmaxf(__uint_as_float(s[0]), __uint_as_float(s[1])); }
DI float wave_sum(float x) { x += dpp<0xB1>(x); x += dpp<0x4E>(x); x += dpp<0x141>(x); x += dpp<0x140>(x); return x32_sum(x16_sum(x)); }

DI void dma_s(u32 ldsaddr, const void* sbase, u32 voff) {
  u32 keep;
  asm volatile("s_mov_b32 %0, m0\n\ts_mov_b32 m0, %1\n\ts_nop 0\n\tglobal_load_lds_dwordx4 %2, %3 nt\n\ts_mov_b32 m0, %0"
               : "=&s"(keep) : "s"(ldsaddr), "v"(voff), "s"(sbase) : "memory");
}
DI void dma_s_keep(u32 ldsaddr, const void* sbase, u32 voff) {
  u32 keep;
  asm volatile("s_mov_b32 %0, m0\n\ts_mov_b32 m0, %1\n\ts_nop 0\n\tglobal_load_lds_dwordx4 %2, %3\n\ts_mov_b32 m0, %0"
               : "=&s"(keep) : "s"(ldsaddr), "v"(voff), "s"(sbase) : "memory");
}
DI void dma_v(u32 ldsaddr, const void* vaddr) {
  u32 keep;
  asm volatile("s_mov_b32 %0, m0\n\ts_mov_b32 m0, %1\n\ts_nop 0\n\tglobal_load_lds_dwordx4 %2, off nt\n\ts_mov_b32 m0, %0"
               : "=&s"(keep) : "s"(ldsaddr), "v"(vaddr) : "memory");
}
DI u32 swz(u32 row) { return ((row & 3u) << 2) | ((row >> 2) & 3u); }
DI v4u ldsr128(const LAS char* p) { return *(const LAS v4u*)p; }
DI v4u tr2(const LAS char* p0, const LAS char* p1) {
  s16x4 a = __builtin_amdgcn_ds_read_tr16_b64_v4i16((LAS s16x4*)p0);
  s16x4 b = __builtin_amdgcn_ds_read_tr16_b64_v4i16((LAS s16x4*)p1);
  s16x8 v = __builtin_shufflevector(a, b, 0, 1, 2, 3, 4, 5, 6, 7);
  return __builtin_bit_cast(v4u, v);
}
DI void wait_ticks(u64 t0, u32 ticks) { if (ticks) { while (__builtin_amdgcn_s_memrealtime() - t0 < ticks) __builtin_amdgcn_s_sleep(2); } }

#define STAMP(i) do { } while (0)
#define STARG

struct Ptrs { const u16 *x, *ckv, *wq, *wuk, *wuv, *wo, *wg, *wu, *wd, *c, *wmod; u16* out; char* ws; const char* code; u64 t0; };

DI v4u frag64(const LAS char* img, int lane, int c, int ks) {
  const int g = lane >> 4, q = (lane & 15) >> 2, p = lane & 3;
  const LAS char* a = img + (32 * ks + 8 * g + q) * 64 + 32 * c + 8 * p;
  return tr2(a, a + 4 * 64);
}
DI v4u frag256(const LAS char* img, int lane, int c, int ks) {
  const int g = lane >> 4, q = (lane & 15) >> 2, p = lane & 3;
  const u32 r0 = 8 * g + q, r1 = r0 + 4, ch = 2 * c + (p >> 1);
  return tr2(img + 8192 * ks + 256 * r0 + 16 * (ch ^ swz(r0)) + 8 * (p & 1), img + 8192 * ks + 256 * r1 + 16 * (ch ^ swz(r1)) + 8 * (p & 1));
}
template <int N> DI f32x4 gather_sum(rsrc_t ws, int off, int stride, u32 T) {
  f32x4 s;
  _Pragma("nounroll") for (u32 sp = 0; sp < SPIN_CAP; ++sp) {
    CBAR();
    v4u t[N];
#pragma unroll
    for (int k = 0; k < N; ++k) t[k] = wld128(ws, off + k * stride);
    u32 bad = 0;
#pragma unroll
    for (int k = 0; k < N; ++k) bad |= tbad4(t[k], T);
    s = asf4(t[0]);
#pragma unroll
    for (int k = 1; k < N; ++k) s += asf4(t[k]);
    if (__all(bad == 0)) break;
  }
  return s;
}
template <int SLEEP> DI void wait_piece(rsrc_t ws, int off, u32 T, int lane) {
  _Pragma("nounroll") for (u32 sp = 0; sp < SPIN_CAP; ++sp) {
    CBAR();
    const v4u f = wld128(ws, lane == 0 ? off : OOB);
    if (__builtin_amdgcn_readfirstlane(tbad4(f, T)) == 0) break;
    __builtin_amdgcn_s_sleep(SLEEP);
  }
}
DI u32 lds_ld(const LAS char* p) { return *(const volatile LAS u32*)p; }
#define PIN(x) asm volatile("" : "+v"(x))
DI void lds_wait_ge(const LAS char* p, u32 n) { _Pragma("nounroll") for (u32 sp = 0; sp < (1u << 22); ++sp) { if ((u32)__builtin_amdgcn_readfirstlane(lds_ld(p)) >= n) break; } CBAR(); }
DI void lds_arrive(LAS char* p, int lane) { asm volatile("s_waitcnt lgkmcnt(0)" ::: "memory"); if (lane == 0) __hip_atomic_fetch_add((LAS u32*)p, 1u, __ATOMIC_RELAXED, __HIP_MEMORY_SCOPE_WORKGROUP); }
#define LGKM0() asm volatile("s_waitcnt lgkmcnt(0)" ::: "memory")

constexpr int L_CNT = LDS_BYTES - 64;
constexpr int FL_WQ = 0, FL_WUK = 40960, FL_WO = 0, FL_WUV = 81920, FL_SM = FL_WUV + 32768;
constexpr int FL_HB = FL_SM, FL_QB = FL_HB + 1024, FL_QA = FL_QB + 512, FL_CRED = FL_QA + 512, FL_CST = FL_CRED + 4096,
              FL_CB = FL_CST + 64, FL_CTX = FL_CB + 512, FL_WRED = FL_CTX + 4096, FL_FIN = FL_WRED + 4096, FL_PF = FL_FIN + 1024,
              FL_SB = FL_PF + 1024  ;
static_assert(FL_PF % 16 == 0 && FL_SB + 2560 <= L_CNT, "lds");

DI void f_role(const Ptrs& P, LAS char* L, rsrc_t ws, int p, int w, int lane STARG) {
  const int i16 = lane & 15, g = lane >> 4;
  const int h = p & 31, sl = p >> 5, cp = p & 7;
  const f32x4 Z4 = {0.f, 0.f, 0.f, 0.f};
  float ln[4] = {0.f, 0.f, 0.f, 0.f};
  u32 T = 0;
  if (w < 2) {
    T = wld32(ws, WS_EPOCH);
    {
      const v4u S = {SENT, SENT, SENT, SENT};
#pragma unroll
      for (int k = 0; k < 2; ++k) wst128(ws, WS_S3 + (4 * w + 2 * k + (lane >> 5)) * S3_COPY + (((lane >> 4) & 1) * 32 + h) * 1024 + sl * 256 + i16 * 16, S);
    }
    const v4u xa = *(const GAS v4u*)(P.x + w * 640 + 8 * lane);
    const v4u xb = *(const GAS v4u*)(P.x + w * 640 + 512 + 8 * (lane & 15));
    const v2u xs = *(const GAS v2u*)(P.x + w * 640 + 160 * sl + 4 * (lane < 40 ? lane : 0));
    VM0();
    T = __builtin_amdgcn_readfirstlane(mk_tag(T));
    { const v2u f = {T, ~T}; wst64(ws, lane == 0 ? WS_FLAGF + p * 128 + 8 * w : OOB, f); }
    if (w == 0 && lane == 0) *(LAS u32*)(L + L_CNT + 4) = T;
    float v[16];
#pragma unroll
    for (int k = 0; k < 4; ++k) { v[2 * k] = bflo(xa[k]); v[2 * k + 1] = bfhi(xa[k]); v[8 + 2 * k] = bflo(xb[k]); v[9 + 2 * k] = bfhi(xb[k]); }
    const bool ex = lane < 16;
    float s = 0.f;
#pragma unroll
    for (int k = 0; k < 8; ++k) s += v[k] + (ex ? v[8 + k] : 0.f);
    const float mean = wave_sum(s) * (1.f / 640.f);
    float s2 = 0.f;
#pragma unroll
    for (int k = 0; k < 8; ++k) { const float d = v[k] - mean, e = v[8 + k] - mean; s2 += d * d + (ex ? e * e : 0.f); }
    const float rstd = rsqrtf(wave_sum(s2) * (1.f / 640.f) + 1e-5f);
    ln[0] = (bflo(xs.x) - mean) * rstd; ln[1] = (bfhi(xs.x) - mean) * rstd; ln[2] = (bflo(xs.y) - mean) * rstd; ln[3] = (bfhi(xs.y) - mean) * rstd;
  } else if (w < 6) {
    const char* sb = (const char*)P.wq + (size_t)(160 * sl) * 8192 + h * 256;
    const u32 n0 = w - 2;
    const u32 vo = g * 8192 + ((i16 ^ ((g << 2) | n0)) << 4);
#pragma unroll 2
    for (int k = 0; k < 10; ++k) dma_s(FL_WQ + (n0 + 4 * k) * 1024, sb + (size_t)(n0 + 4 * k) * 4 * 8192, vo);
    vmwait<0>();
    lds_arrive(L + L_CNT, lane);
  } else {
    __builtin_amdgcn_s_sleep(2);
    const char* su = (const char*)P.wuk + (size_t)(128 * sl) * 8192 + h * 256;
    const u32 n0 = w - 6;
    const u32 vo0 = g * 8192 + ((i16 ^ ((g << 2) | n0)) << 4), vo1 = g * 8192 + ((i16 ^ ((g << 2) | (n0 + 2))) << 4);
#pragma unroll 1
    for (int k = 0; k < 8; ++k) {
      dma_s(FL_WUK + (n0 + 4 * k) * 1024, su + (size_t)(n0 + 4 * k) * 4 * 8192, vo0);
      dma_s(FL_WUK + (n0 + 4 * k + 2) * 1024, su + (size_t)(n0 + 4 * k + 2) * 4 * 8192, vo1);
    }
    if (w == 7) {
      const int o0 = (p >> 3) * 1024;
      dma_s_keep(FL_PF, P.code + (o0 < CODE_PF - 1024 ? o0 : CODE_PF - 1024), lane * 16);
      dma_s_keep(FL_PF, P.code + (o0 + 16384 < CODE_PF - 1024 ? o0 + 16384 : CODE_PF - 1024), lane * 16);
    }
  }
  STAMP(1);
  u32 hb_a = FL_HB + (lane & 1) * 320 + g * 16, s2_o = i16 < 2 ? WS_S2 + (h * 4 + sl) * 1024 + w * 128 + i16 * 64 + g * 16 : OOB;
  PIN(hb_a); PIN(s2_o);
  lds_wait_ge(L + L_CNT, 4u);
  v4u aq[5];
#pragma unroll
  for (int k = 0; k < 5; ++k) aq[k] = frag256(L + FL_WQ, lane, w, k);
  STAMP(2);
  if (w < 2) {
    f32x4 sa, ca;
    const int s1_o = lane < 40 ? WS_S1 + cp * S1_COPY + (w * 640 + 160 * sl + 4 * lane) * 4 : OOB;
    _Pragma("nounroll") for (u32 sp = 0; sp < SPIN_CAP; ++sp) {
      CBAR();
      v4u t[10];
#pragma unroll
      for (int k = 0; k < 10; ++k) t[k] = wld128(ws, s1_o + ((k >> 1) * 4 + (k & 1) * 2) * 2560);
      u32 bad = 0;
#pragma unroll
      for (int k = 0; k < 10; ++k) bad |= tbad4(t[k], T);
      sa = asf4(t[0]); ca = asf4(t[1]);
#pragma unroll
      for (int k = 1; k < 5; ++k) { sa += asf4(t[2 * k]); ca += asf4(t[2 * k + 1]); }
      if (__all(lane >= 40 || bad == 0)) break;
    }
    const float h0 = ln[0] * (1.f + ca[0]) + sa[0], h1 = ln[1] * (1.f + ca[1]) + sa[1];
    const float h2 = ln[2] * (1.f + ca[2]) + sa[2], h3 = ln[3] * (1.f + ca[3]) + sa[3];
    if (lane < 40) { const v2u o = {pk_bf16(h0, h1), pk_bf16(h2, h3)}; *(LAS v2u*)(L + FL_HB + w * 320 + lane * 8) = o; }
    STAMP(4);
  }
  BAR();
  {
    v4u bh[5];
#pragma unroll
    for (int k = 0; k < 5; ++k) bh[k] = ldsr128(L + hb_a + 64 * k);
    T = __builtin_amdgcn_readfirstlane(lds_ld(L + L_CNT + 4));
    f32x4 acc = Z4;
#pragma unroll
    for (int k = 0; k < 5; ++k) acc = mfma16(aq[k], bh[k], acc);
    wst128(ws, s2_o, tag4(acc, T));
  }
  STAMP(5);
  u32 qb_a = FL_QB + (lane & 1) * 256 + g * 16, uk_a[4], qa_w = FL_QA + i16 * 256 + (16 * w + 4 * g) * 2;
  {
    const u32 row = 16 * w + i16, sw = swz(row);
#pragma unroll
    for (int k = 0; k < 4; ++k) { uk_a[k] = FL_WUK + row * 256 + (((4 * k + g) ^ sw) << 4); PIN(uk_a[k]); }
  }
  PIN(qb_a); PIN(qa_w);
  if (w == 0) {
    const f32x4 q = gather_sum<4>(ws, WS_S2 + h * 4096 + lane * 16, 1024, T);
    const v2u o = {pk_bf16(q[0], q[1]), pk_bf16(q[2], q[3])};
    *(LAS v2u*)(L + FL_QB + ((lane >> 2) & 1) * 256 + (16 * (lane >> 3) + 4 * (lane & 3)) * 2) = o;
    STAMP(6);
  } else if (w >= 6) {
    vmwait<0>();
  }
  BAR();
  {
    v4u bq[4], au[4];
#pragma unroll
    for (int k = 0; k < 4; ++k) { au[k] = ldsr128(L + uk_a[k]); bq[k] = ldsr128(L + qb_a + 64 * k); }
    f32x4 acc = Z4;
#pragma unroll
    for (int k = 0; k < 4; ++k) acc = mfma16(au[k], bq[k], acc);
    constexpr float SC = 0.08838834764831845f * 1.4426950408889634f;
    if (i16 < 2) { const v2u o = {canon(pk_bf16(acc[0] * SC, acc[1] * SC)), canon(pk_bf16(acc[2] * SC, acc[3] * SC))};
      *(LAS v2u*)(L + qa_w) = o; }
  }
  BAR();
  wst128(ws, lane < 32 ? WS_S3 + w * S3_COPY + ((lane >> 4) * 32 + h) * 1024 + sl * 256 + i16 * 16 : OOB, ldsr128(L + FL_QA + (lane & 31) * 16));
  STAMP(7);
  const int kq = p / 10, ms = p % 10;
  if (w >= 4) {
    if (p < 88) wait_ticks(P.t0, F_TAIL_DELAY);
    const u32 n0 = w - 4;
    const char* sb = (const char*)P.wuv + (size_t)(128 * sl) * 8192 + h * 256;
    const u32 vo = g * 8192 + ((i16 ^ ((g << 2) | n0)) << 4);
#pragma unroll 2
    for (int k = 0; k < 8; ++k) dma_s(FL_WUV + (n0 + 4 * k) * 1024, sb + (size_t)(n0 + 4 * k) * 4 * 8192, vo);
    if (p < 80) {
      const char* so = (const char*)P.wo + (size_t)(512 * kq) * 1280 + ms * 128;
      const u32 vo2 = (lane >> 3) * 1280 + (lane & 7) * 16;
#pragma unroll 2
      for (int n = n0; n < 64; n += 4) dma_s(FL_WO + n * 1024, so + (size_t)n * 8 * 1280, vo2);
    } else if (p >= 88) {
      const char* sm = (const char*)P.wmod + (size_t)(1280 + 64 * (p - 88)) * 2;
      const u32 vo2 = (lane >> 3) * 7680 + (lane & 7) * 16;
#pragma unroll 2
      for (int n = n0; n < 80; n += 4) dma_s(FL_WO + n * 1024, sm + (size_t)n * 8 * 7680, vo2);
    }
    vmwait<0>();
  } else if (p >= 88 && w < 3) {
    const int tid = w * 64 + lane;
    const v4u cvv = *(const GAS v4u*)(P.c + (tid < 160 ? tid : 0) * 8);
    v4u sv;
#pragma unroll
    for (int k = 0; k < 4; ++k) sv[k] = pk_bf16(silu_f(bflo(cvv[k])), silu_f(bfhi(cvv[k])));
    if (tid < 160) *(LAS v4u*)(L + FL_SB + tid * 16) = sv;
  }
  BAR();
  STAMP(8);
  if (p >= 88) {
    f32x4 a[4] = {Z4, Z4, Z4, Z4};
    const int q = (lane & 15) >> 2, pp = lane & 3;
    for (int ks = w; ks < 20; ks += 8) {
      const v4u b = ldsr128(L + FL_SB + (lane & 1) * 1280 + (32 * ks + 8 * g) * 2);
      const LAS char* base = L + FL_WO + (32 * ks + 8 * g + q) * 128 + 8 * pp;
#pragma unroll
      for (int c = 0; c < 4; ++c) a[c] = mfma16(tr2(base + 32 * c, base + 32 * c + 4 * 128), b, a[c]);
    }
    if (i16 < 2) {
      LAS f32x4* wr = (LAS f32x4*)(L + FL_WRED + w * 512 + i16 * 256 + g * 16);
#pragma unroll
      for (int c = 0; c < 4; ++c) wr[4 * c] = a[c];
    }
    BAR();
    if (w == 0) {
      const LAS f32x4* wr = (const LAS f32x4*)(L + FL_WRED + (lane & 31) * 16);
      f32x4 v = wr[0];
#pragma unroll
      for (int k = 1; k < 8; ++k) v += wr[32 * k];
      const int col = 64 * (p - 88);
      wst128(ws, lane < 32 ? WS_MB + (((col / 640) * 2 + (lane >> 4)) * 640 + col % 640) * 4 + (lane & 15) * 16 : OOB, tag4(v, T));
    }
  }
  v4u uv[4], ao[8];
#pragma unroll
  for (int k = 0; k < 4; ++k) uv[k] = frag256(L + FL_WUV, lane, w, k);
  if (p < 80) {
    const LAS char* base = L + FL_WO + (64 * w + 8 * g + ((lane & 15) >> 2)) * 128 + 8 * (lane & 3);
#pragma unroll
    for (int k = 0; k < 8; ++k) { const LAS char* a = base + (k >> 2) * 32 * 128 + (k & 3) * 32; ao[k] = tr2(a, a + 4 * 128); }
  }
  {
    const int b = w >> 2, k0 = 16 * (w & 3);
    const int sto = WS_ST + ((b * 64 + k0 + i16) * 8 + (h & 7)) * 128 + (h >> 4) * 16;
    int s4_o = WS_S4 + ((((h * 4 + sl) * 2 + b) * 64 + k0) * 256) + lane * 16;
    u32 cb_a = FL_CB + (lane & 1) * 256 + g * 16, s5_o = i16 < 2 ? WS_S5 + (h * 4 + sl) * 1024 + w * 128 + i16 * 64 + g * 16 : OOB, cr_a = FL_CRED + w * 512 + (lane & 15) * 32;
    PIN(s4_o); PIN(cb_a); PIN(s5_o); PIN(cr_a);
    if (w == 0) wait_piece<8>(ws, sto, T, lane);
    BAR();
    v4u s;
    _Pragma("nounroll") for (u32 sp = 0; sp < SPIN_CAP; ++sp) {
      CBAR();
      s = wld128(ws, lane < 16 ? sto : OOB);
      if (__all(lane >= 16 || tbad4(s, T) == 0)) break;
      __builtin_amdgcn_s_sleep(2);
    }
    float wj[4];
    {
      const bool odd = (h >> 3) & 1;
      const float m = __uint_as_float((odd ? s.z : s.x) & 0xFFFFFF00u), l = __uint_as_float((odd ? s.w : s.y) & 0xFFFFFF00u);
      float Mw = fmaxf(m, dpp<0xB1>(m)); Mw = fmaxf(Mw, dpp<0x4E>(Mw)); Mw = fmaxf(Mw, dpp<0x141>(Mw)); Mw = fmaxf(Mw, dpp<0x140>(Mw));
      const float e = __builtin_amdgcn_exp2f(m - Mw);
      float Lw = e * l; Lw += dpp<0xB1>(Lw); Lw += dpp<0x4E>(Lw); Lw += dpp<0x141>(Lw); Lw += dpp<0x140>(Lw);
      if (lane == 0) *(LAS f32x2*)(L + FL_CST + w * 8) = f32x2{Mw, Lw};
#pragma unroll
      for (int j = 0; j < 4; ++j) wj[j] = __shfl(e, 4 * j + g);
    }
    STAMP(9);
    __builtin_amdgcn_s_sleep(S4_SLEEP);
    v4u t[4];
    _Pragma("nounroll") for (u32 sp = 0; sp < SPIN_CAP; ++sp) {
      CBAR();
#pragma unroll
      for (int j = 0; j < 4; ++j) t[j] = wld128(ws, s4_o + j * 1024);
      u32 mx = 0;
#pragma unroll
      for (int j = 0; j < 4; ++j) mx = max(mx, max(max(t[j].x, t[j].y), max(t[j].z, t[j].w)));
      if (__all(mx != SENT)) break;
    }
    STAMP(3);
    float acc[8];
#pragma unroll
    for (int k = 0; k < 4; ++k) { acc[2 * k] = wj[0] * bflo(t[0][k]); acc[2 * k + 1] = wj[0] * bfhi(t[0][k]); }
#pragma unroll
    for (int j = 1; j < 4; ++j) {
#pragma unroll
      for (int k = 0; k < 4; ++k) { acc[2 * k] += wj[j] * bflo(t[j][k]); acc[2 * k + 1] += wj[j] * bfhi(t[j][k]); }
    }
#pragma unroll
    for (int k = 0; k < 8; ++k) acc[k] = x32_sum(x16_sum(acc[k]));
    if (lane < 16) {
      LAS f32x4* cr = (LAS f32x4*)(L + cr_a);
      cr[0] = f32x4{acc[0], acc[1], acc[2], acc[3]}; cr[1] = f32x4{acc[4], acc[5], acc[6], acc[7]};
    }
    BAR();
    if (w == 0) {
      const int bb = lane >> 5;
      const LAS f32x4* cs = (const LAS f32x4*)(L + FL_CST + bb * 32);
      const f32x4 c01 = cs[0], c23 = cs[1];
      const LAS f32x4* cr = (const LAS f32x4*)(L + FL_CRED + bb * 2048 + (lane & 31) * 16);
      const f32x4 r0 = cr[0], r1 = cr[32], r2 = cr[64], r3 = cr[96];
      const float M = fmaxf(fmaxf(c01[0], c01[2]), fmaxf(c23[0], c23[2]));
      const float f0 = __builtin_amdgcn_exp2f(c01[0] - M), f1 = __builtin_amdgcn_exp2f(c01[2] - M), f2 = __builtin_amdgcn_exp2f(c23[0] - M), f3 = __builtin_amdgcn_exp2f(c23[2] - M);
      const float inv = __builtin_amdgcn_rcpf((f0 * c01[1] + f1 * c01[3]) + (f2 * c23[1] + f3 * c23[3]));
      const f32x4 v = ((r0 * f0 + r1 * f1) + (r2 * f2 + r3 * f3)) * inv;
      const v2u o = {pk_bf16(v[0], v[1]), pk_bf16(v[2], v[3])};
      *(LAS v2u*)(L + FL_CB + lane * 8) = o;
    }
    BAR();
    v4u bc[4];
#pragma unroll
    for (int k = 0; k < 4; ++k) bc[k] = ldsr128(L + cb_a + 64 * k);
    f32x4 a2 = Z4;
#pragma unroll
    for (int k = 0; k < 4; ++k) a2 = mfma16(uv[k], bc[k], a2);
    wst128(ws, s5_o, tag4(a2, T));
  }
  STAMP(10);
  if (p < 80) {
    u32 cx_w = FL_CTX + w * 256 + ((lane >> 2) & 1) * 128 + (16 * ((lane & 31) >> 3) + 4 * (lane & 3)) * 2, cx_r = FL_CTX + w * 256 + (lane & 1) * 128 + g * 16,
        wr_w = FL_WRED + w * 512 + (i16 & 1) * 256 + g * 16, wr_r = FL_WRED + (lane & 31) * 16,
        s6_o = lane < 32 ? WS_S6 + w * S6_COPY + ((kq * 2 + (lane >> 4)) * 640 + 64 * ms) * 4 + (lane & 15) * 16 : OOB;
    PIN(cx_w); PIN(cx_r); PIN(wr_w); PIN(wr_r); PIN(s6_o);
    f32x4 cx = gather_sum<2>(ws, WS_S5 + (4 * kq + (w >> 1)) * 4096 + (lane >> 5) * 1024 + (w & 1) * 512 + (lane & 31) * 16, 2048, T);
    STAMP(11);
#pragma unroll
    for (int k = 0; k < 4; ++k) cx[k] = x32_sum(cx[k]);
    const v2u o = {pk_bf16(cx[0], cx[1]), pk_bf16(cx[2], cx[3])};
    if (lane < 32) *(LAS v2u*)(L + cx_w) = o;
    LGKM0();
    v4u bx[2];
#pragma unroll
    for (int k = 0; k < 2; ++k) bx[k] = ldsr128(L + cx_r + 64 * k);
    f32x4 a[4];
#pragma unroll
    for (int c = 0; c < 4; ++c) a[c] = mfma16(ao[4 + c], bx[1], mfma16(ao[c], bx[0], Z4));
    if (i16 < 2) {
      LAS f32x4* wr = (LAS f32x4*)(L + wr_w);
#pragma unroll
      for (int c = 0; c < 4; ++c) wr[4 * c] = a[c];
    }
    BAR();
    {
      const LAS f32x4* wr = (const LAS f32x4*)(L + wr_r);
      f32x4 v[8];
#pragma unroll
      for (int k = 0; k < 8; ++k) v[k] = wr[32 * k];
      const f32x4 r = ((v[0] + v[1]) + (v[2] + v[3])) + ((v[4] + v[5]) + (v[6] + v[7]));
      wst128(ws, s6_o, tag4(r, T));
    }
    STAMP(12);
  } else if (p >= 88) {
    const int fs = p - 88, bm = lane & 7, bb = bm >> 2, m0 = 16 * fs + 4 * (bm & 3);
    f32x4 x1, gb;
    if (w == 0) {
      const v2u xv = *(const GAS v2u*)(P.x + bb * 640 + m0);
      _Pragma("nounroll") for (u32 sp = 0; sp < SPIN_CAP; ++sp) {
        CBAR();
        const v4u t0 = wld128(ws, WS_S6 + cp * S6_COPY + (((lane >> 3) * 2 + bb) * 640 + m0) * 4);
        const v4u t4 = wld128(ws, lane < 8 ? WS_MB + ((0 * 2 + bb) * 640 + m0) * 4 : OOB);
        const v4u t5 = wld128(ws, lane < 8 ? WS_MB + ((3 * 2 + bb) * 640 + m0) * 4 : OOB);
        const u32 bad = tbad4(t0, T) | (lane < 8 ? tbad4(t4, T) | tbad4(t5, T) : 0u);
        f32x4 at = asf4(t0);
#pragma unroll
        for (int k = 0; k < 4; ++k) { float v = at[k]; v += dpp<0x128>(v); at[k] = x32_sum(x16_sum(v)); }
        const f32x4 xf = {bflo(xv.x), bfhi(xv.x), bflo(xv.y), bfhi(xv.y)};
        x1 = xf + asf4(t4) * at;
        gb = asf4(t5);
        if (__all(bad == 0)) break;
        __builtin_amdgcn_s_sleep(4);
      }
    }
    BAR();
    STAMP(11);
    f32x4 y;
    const int tid = w * 64 + lane;
    _Pragma("nounroll") for (u32 sp = 0; sp < SPIN_CAP; ++sp) {
      CBAR();
      const v4u t0 = wld128(ws, WS_S8 + fs * (108 * 128) + tid * 16);
      const v4u t1 = wld128(ws, WS_S8 + fs * (108 * 128) + (tid < 352 ? 512 + tid : tid) * 16);
      y = asf4(t0);
      if (tid < 352) y += asf4(t1);
      if (__all((tbad4(t0, T) | tbad4(t1, T)) == 0)) break;
      __builtin_amdgcn_s_sleep(1);
    }
    STAMP(12);
#pragma unroll
    for (int k = 0; k < 4; ++k) { float v = y[k]; v += dpp<0x128>(v); y[k] = x32_sum(x16_sum(v)); }
    if (lane < 8) *(LAS f32x4*)(L + FL_FIN + w * 128 + lane * 16) = y;
    BAR();
    if (w == 0) {
      const LAS f32x4* fr = (const LAS f32x4*)(L + FL_FIN + bm * 16);
      f32x4 v[8];
#pragma unroll
      for (int k = 0; k < 8; ++k) v[k] = fr[8 * k];
      const f32x4 o = x1 + gb * (((v[0] + v[1]) + (v[2] + v[3])) + ((v[4] + v[5]) + (v[6] + v[7])));
      const v2u ob = {pk_bf16(o[0], o[1]), pk_bf16(o[2], o[3])};
      if (lane < 8) *(GAS v2u*)(P.out + bb * 640 + m0) = ob;
      if (p == 88 && lane == 0) wst32(ws, WS_EPOCH, wld32(ws, WS_EPOCH) + 1u);
    }
    STAMP(13);
  }
}

constexpr int AL_CKV = 0, AL_Q = 131072;
constexpr int AL_P = AL_Q, AL_MAX = AL_Q + 8192, AL_SUM = AL_MAX + 512;
constexpr int AL_O = 0, AL_GU = 32768, AL_DN = AL_Q + 10240  , AL_SM = AL_GU + 40960;
constexpr int AL_H2 = AL_SM, AL_STAT = AL_H2 + 2560, AL_RED = AL_STAT + 64, AL_ACT = AL_RED + 1024;
constexpr int AL_CNT = AL_Q + 4096;
static_assert(AL_ACT + 8 * 128 <= AL_Q && AL_SUM + 512 <= AL_DN && AL_DN + 20480 <= LDS_BYTES, "lds");

DI void a_role(const Ptrs& P, LAS char* L, rsrc_t ws, int j, int w, int lane STARG) {
  const int i16 = lane & 15, g = lane >> 4, l32 = lane & 31, hh = lane >> 5;
  const int b = j >> 6, kc = j & 63, cp = j & 7;
  const f32x4 Z4 = {0.f, 0.f, 0.f, 0.f};
  f32x16 S;
  const int fl = (j & 7) + 8 * (j >> 5), fsub = (j >> 3) & 3, fj = 4 * fl + fsub;
  const bool do_ffn = fl < 27;
  v4u dummy = {0u, 0u, 0u, 0u};
  u32 T = 0;
  u32 ca[8], qa[8];
  T = wld32(ws, WS_EPOCH);
  {
    const int un = (j & 7) + 8 * (j >> 4), cg = 2 * (un / 5) + ((j >> 3) & 1), ks1 = un % 5;
    if (un < 50) {
      const int base = 128 * ks1 + 16 * w;
      const int bb = (lane >> 3) & 1, r1 = i16 >> 3, mat = (i16 >> 2) & 1, ch = i16 & 3, jj = lane & 7;
      u32 cv[2]; v4u wm[2];
#pragma unroll
      for (int t = 0; t < 2; ++t) cv[t] = *(const GAS u32*)(P.c + bb * 640 + base + 8 * t + 2 * g);
#pragma unroll
      for (int t = 0; t < 2; ++t) wm[t] = ldnt128(P.wmod + (size_t)(base + 8 * t + 2 * g + r1) * 3840 + mat * 640 + 32 * cg + 8 * ch);
      u32 msk[4];
#pragma unroll
      for (int r = 0; r < 4; ++r) msk[r] = (r == (jj >> 1)) ? (0xFFFFu << (16 * (jj & 1))) : 0u;
      int s1_o = WS_S1 + w * S1_COPY + ((ks1 * 4 + (l32 >> 3)) * 640 + 32 * cg) * 4 + (lane & 7) * 16;
      if (lane >= 32) s1_o = OOB;
      PIN(s1_o);
      f32x4 a0 = Z4, a1 = Z4;
#pragma unroll
      for (int t = 0; t < 2; ++t) {
        const u32 sp = pk_bf16(silu_f(bflo(cv[t])), silu_f(bfhi(cv[t])));
        const u32 d0 = (sp & 0xFFFFu) * 0x10001u, d1 = (sp >> 16) * 0x10001u;
        const v4u B0 = {d0 & msk[0], d0 & msk[1], d0 & msk[2], d0 & msk[3]};
        const v4u B1 = {d1 & msk[0], d1 & msk[1], d1 & msk[2], d1 & msk[3]};
        a0 = mfma16(wm[t], B0, a0);
        a1 = mfma16(wm[t], B1, a1);
      }
      LAS float* red = (LAS float*)(L + AL_Q) + w * 128;
      const int mt = (lane >> 4) & 1;
#pragma unroll
      for (int r = 0; r < 4; ++r) {
        const float v = x32_sum(lane < 32 ? a0[r] : a1[r]);
        if (lane < 32) red[(mt * 2 + bb) * 32 + 8 * r + jj] = v;
      }
      T = __builtin_amdgcn_readfirstlane(mk_tag(T));
      BAR();
      {
        const LAS f32x4* rd = (const LAS f32x4*)(L + AL_Q) + l32;
        const f32x4 s = ((rd[0] + rd[32]) + (rd[64] + rd[96])) + ((rd[128] + rd[160]) + (rd[192] + rd[224]));
        wst128(ws, s1_o, tag4(s, T));
      }
      BAR();
    } else {
      T = __builtin_amdgcn_readfirstlane(mk_tag(T));
    }
  }
  STAMP(14);
  if (w >= 4) {
    const int lw = w - 4;
    const char* sb = (const char*)P.ckv + ((size_t)(b * 8192 + kc * 128 + 32 * lw)) * 1024;
    u32 vo[4];
#pragma unroll
    for (int i = 0; i < 4; ++i) vo[i] = g * 1024 + ((i16 ^ ((g << 2) | i)) << 4);
    wait_ticks(P.t0, A_DELAY);
    for (int cq = 0; cq < 4; ++cq) {
#pragma unroll
      for (int i = 0; i < 8; ++i) dma_s(AL_CKV + cq * 32768 + (8 * lw + i) * 1024, sb + i * 4096 + cq * 256, vo[i & 3]);
    }
    STAMP(1);
    vmwait<0>();
  } else {
    {
      const v4u Sv = {SENT, SENT, SENT, SENT};
#pragma unroll
      for (int k = 0; k < 8; ++k) wst128(ws, WS_S4 + (((((8 * w + k) * 4 + g) * 2 + b) * 64 + kc) * 256) + i16 * 16, Sv);
    }
    {
      const u32 row = 32 * w + l32, sw = swz(row);
#pragma unroll
      for (int s = 0; s < 8; ++s) { ca[s] = AL_CKV + row * 256 + (((2 * s + hh) ^ sw) << 4); qa[s] = AL_Q + l32 * 1024 + (((2 * s + hh) ^ (l32 & 15)) << 4); PIN(ca[s]); PIN(qa[s]); }
    }
    _Pragma("nounroll") for (u32 sp = 0; sp < SPIN_CAP; ++sp) {
      CBAR();
      const v4u f = wld128(ws, lane < 32 ? WS_FLAGF + (8 * w + (lane & 7) + 32 * (lane >> 3)) * 128 : OOB);
      if (__all(lane >= 32 || ((f.x ^ T) | (f.y ^ ~T) | (f.z ^ T) | (f.w ^ ~T)) == 0)) break;
      __builtin_amdgcn_s_sleep(8);
    }
    STAMP(1);
    const int s3_o = WS_S3 + cp * S3_COPY + (b * 32 + 8 * w) * 1024;
    _Pragma("nounroll") for (u32 sp = 0; sp < SPIN_CAP; ++sp) {
      CBAR();
      const u32 f = wld32(ws, lane == 0 ? s3_o : OOB);
      if (__builtin_amdgcn_readfirstlane(f) != SENT) break;
      __builtin_amdgcn_s_sleep(4);
    }
    _Pragma("nounroll") for (u32 sp = 0; sp < SPIN_CAP; ++sp) {
      CBAR();
      v4u t[8];
#pragma unroll
      for (int k = 0; k < 8; ++k) t[k] = wld128(ws, s3_o + k * 1024 + lane * 16);
      u32 mx = 0;
#pragma unroll
      for (int k = 0; k < 8; ++k) { const u32 a = max(max(t[k].x, t[k].y), max(t[k].z, t[k].w)); mx = max(mx, a); }
      if (__all(mx != SENT)) {
#pragma unroll
        for (int k = 0; k < 8; ++k) { const int hd = 8 * w + k; *(LAS v4u*)(L + AL_Q + hd * 1024 + ((lane ^ (hd & 15)) << 4)) = t[k]; }
        break;
      }
      __builtin_amdgcn_s_sleep(2);
    }
    VM0();
    STAMP(2);
  }
  BAR();
  if (w < 4) {
#pragma unroll
    for (int k = 0; k < 16; ++k) S[k] = 0.f;
    {
      v4u a[32], q[32];
#pragma unroll
      for (int s = 0; s < 6; ++s) { a[s] = ldsr128(L + ca[s & 7] + (s >> 3) * 32768); q[s] = ldsr128(L + qa[s & 7] + (s >> 3) * 256); }
      __builtin_amdgcn_sched_barrier(0);
#pragma unroll
      for (int s = 0; s < 32; ++s) {
        if (s + 6 < 32) { const int n = s + 6; a[n] = ldsr128(L + ca[n & 7] + (n >> 3) * 32768); q[n] = ldsr128(L + qa[n & 7] + (n >> 3) * 256); }
        S = mfma32(a[s], q[s], S);
        __builtin_amdgcn_sched_barrier(0);
      }
    }
    STAMP(3);
    float m0 = fmaxf(fmaxf(S[0], S[1]), fmaxf(S[2], S[3])), m1 = fmaxf(fmaxf(S[4], S[5]), fmaxf(S[6], S[7]));
    float m2 = fmaxf(fmaxf(S[8], S[9]), fmaxf(S[10], S[11])), m3 = fmaxf(fmaxf(S[12], S[13]), fmaxf(S[14], S[15]));
    const float m = x32_max(fmaxf(fmaxf(m0, m1), fmaxf(m2, m3)));
    if (lane < 32) *(LAS float*)(L + AL_MAX + (w * 32 + lane) * 4) = m;
  }
  u32 pa[8], va[4], oa[8];
  {
    const int blk = (lane >> 4) & 1, q = (lane & 15) >> 2, pp = lane & 3;
    const u32 sw = swz(l32);
    const u32 c0 = 8 * (w & 1) + 2 * blk + (pp >> 1);
    const u32 r0 = 8 * hh + q, r1 = r0 + 4, s0 = swz(r0), s1 = swz(r1);
    const u32 tb = AL_CKV + (w >> 1) * 32768 + 8 * (pp & 1);
    va[0] = tb + 256 * r0 + 16 * (c0 ^ s0); va[1] = tb + 256 * r1 + 16 * (c0 ^ s1);
    va[2] = tb + 256 * r0 + 16 * ((c0 + 4) ^ s0); va[3] = tb + 256 * r1 + 16 * ((c0 + 4) ^ s1);
#pragma unroll
    for (int k = 0; k < 8; ++k) { pa[k] = AL_P + l32 * 256 + (((2 * k + hh) ^ sw) << 4); oa[k] = AL_O + l32 * 1024 + (((8 * w + k) ^ (l32 & 15)) << 4) + 8 * hh; PIN(pa[k]); PIN(oa[k]); }
#pragma unroll
    for (int k = 0; k < 4; ++k) PIN(va[k]);
  }
  BAR();
  float M;
  {
    const LAS float* mx = (const LAS float*)(L + AL_MAX) + l32;
    M = fmaxf(fmaxf(mx[0], mx[32]), fmaxf(mx[64], mx[96]));
    M = __uint_as_float(__float_as_uint(M) & 0xFFFFFF00u);
  }
  if (w < 4) {
    float sum = 0.f;
#pragma unroll
    for (int k = 0; k < 16; ++k) { S[k] = __builtin_amdgcn_exp2f(S[k] - M); sum += S[k]; }
    const u32 sw = swz(l32);
#pragma unroll
    for (int qd = 0; qd < 4; ++qd) {
      const v2u o = {pk_bf16(S[4 * qd], S[4 * qd + 1]), pk_bf16(S[4 * qd + 2], S[4 * qd + 3])};
      *(LAS v2u*)(L + AL_P + l32 * 256 + (((4 * w + qd) ^ sw) << 4) + 8 * hh) = o;
    }
    sum = x32_sum(sum);
    if (lane < 32) *(LAS float*)(L + AL_SUM + (w * 32 + lane) * 4) = sum;
  }
  BAR();
  STAMP(4);
  f32x16 O0, O1;
#pragma unroll
  for (int k = 0; k < 16; ++k) { O0[k] = 0.f; O1[k] = 0.f; }
  {
    v4u pb[8], a0[8], a1[8];
#pragma unroll
    for (int ks = 0; ks < 8; ++ks) {
      pb[ks] = ldsr128(L + pa[ks]);
      a0[ks] = tr2(L + va[0] + ks * 4096, L + va[1] + ks * 4096);
      a1[ks] = tr2(L + va[2] + ks * 4096, L + va[3] + ks * 4096);
    }
    if (w == 7) {
      const LAS float* sm = (const LAS float*)(L + AL_SUM) + l32;
      const float Ls = (sm[0] + sm[32]) + (sm[64] + sm[96]);
      const float M2 = dpp<0x128>(M), L2 = dpp<0x128>(Ls);
      wst128(ws, (lane < 32 && !(lane & 8)) ? WS_ST + ((b * 64 + kc) * 8 + (lane & 7)) * 128 + (lane >> 4) * 16 : OOB, tag4(f32x4{M, Ls, M2, L2}, T));
    }
    __builtin_amdgcn_sched_barrier(0);
#pragma unroll
    for (int ks = 0; ks < 8; ++ks) { O0 = mfma32(a0[ks], pb[ks], O0); O1 = mfma32(a1[ks], pb[ks], O1); }
  }
  int s4_o = WS_S4 + ((((4 * w * 4 + g) * 2 + b) * 64 + kc) * 256) + i16 * 16;
  u32 or_a[4];
#pragma unroll
  for (int k = 0; k < 4; ++k) { const int hd = 4 * w + k; or_a[k] = AL_O + hd * 1024 + ((lane ^ (hd & 15)) << 4); PIN(or_a[k]); }
  PIN(s4_o);
  BAR();
  STAMP(5);
#pragma unroll
  for (int qd = 0; qd < 4; ++qd) {
    const v2u o0 = {canon(pk_bf16(O0[4 * qd], O0[4 * qd + 1])), canon(pk_bf16(O0[4 * qd + 2], O0[4 * qd + 3]))};
    const v2u o1 = {canon(pk_bf16(O1[4 * qd], O1[4 * qd + 1])), canon(pk_bf16(O1[4 * qd + 2], O1[4 * qd + 3]))};
    *(LAS v2u*)(L + oa[qd]) = o0;
    *(LAS v2u*)(L + oa[4 + qd]) = o1;
  }
  BAR();
  {
    v4u v[4];
#pragma unroll
    for (int k = 0; k < 4; ++k) v[k] = ldsr128(L + or_a[k]);
#pragma unroll
    for (int k = 0; k < 4; ++k) wst128(ws, s4_o + k * (4 * 2 * 64 * 256), v[k]);
  }
  STAMP(6);
  if (!do_ffn) return;
  const int bb = w >> 2, m0 = 160 * (w & 3) + 4 * (lane < 40 ? lane : 0);
  VM0();
  BAR();
  {
#pragma unroll 1
    for (int n = w; n < 40; n += 8) {
      const int line = n * 8 + (lane >> 3), mat = line >= 160, row = 160 * fsub + line - 160 * mat;
      const char* pp = (const char*)(mat ? P.wu : P.wg) + (size_t)row * 3456 + fl * 128 + (lane & 7) * 16;
      asm volatile("global_load_dwordx4 %0, %1, off" : "+v"(dummy) : "v"(pp) : "memory");
    }
    const char* sd = (const char*)P.wd + (size_t)(16 * fj) * 1280;
    for (int n = w; n < 20; n += 8) dma_s(AL_DN + n * 1024, sd + n * 1024, lane * 16);
    vmwait<0>();
    asm volatile("" :: "v"(dummy));
    const char* gp = (const char*)(((lane & 2) ? P.wu : P.wg)) + (size_t)(lane >> 2) * 3456 + fj * 32 + (lane & 1) * 16;
    for (int n = w; n < 40; n += 8) dma_v(AL_GU + n * 1024, gp + (size_t)n * 16 * 3456);
    vmwait<0>();
  }
  BAR();
  v4u ag[5], ad[5];
  {
#pragma unroll
    for (int i = 0; i < 5; ++i) ag[i] = frag64(L + AL_GU, lane, w & 1, (w >> 1) + 4 * i);
    const int q = (lane & 15) >> 2, pp = lane & 3;
    const int r0 = (8 * g + q) & 15;
#pragma unroll
    for (int k = 0; k < 5; ++k) { const LAS char* a = L + AL_DN + r0 * 1280 + (5 * w + k) * 32 + 8 * pp; ad[k] = tr2(a, a + 4 * 1280); }
  }
  STAMP(7);
  {
    const v2u xv = *(const GAS v2u*)(P.x + bb * 640 + m0);
    v4u mb[3];
    _Pragma("nounroll") for (u32 sp = 0; sp < SPIN_CAP; ++sp) {
      CBAR();
#pragma unroll
      for (int k = 0; k < 3; ++k) mb[k] = wld128(ws, lane < 40 ? WS_MB + ((k * 2 + bb) * 640 + m0) * 4 : OOB);
      if (__all(lane >= 40 || (tbad4(mb[0], T) | tbad4(mb[1], T) | tbad4(mb[2], T)) == 0)) break;
      __builtin_amdgcn_s_sleep(8);
    }
    const f32x4 xf = {bflo(xv.x), bfhi(xv.x), bflo(xv.y), bfhi(xv.y)};
    const f32x4 gav = asf4(mb[0]), sbv = asf4(mb[1]), cb1 = asf4(mb[2]) + 1.f;
    u32 h2_w = AL_H2 + (bb * 640 + m0) * 2, h2_r = AL_H2 + (lane & 1) * 1280 + (32 * (w >> 1) + 8 * g) * 2, red_w = AL_RED + w * 128 + (i16 & 1) * 64 + g * 16,
        red_r = AL_RED + (((lane >> 4) & 1) * 16 + (lane & 15)) * 4, act_w = AL_ACT + w * 128 + (((lane >> 4) & 1) * 32 + (lane >> 5) * 16 + (lane & 15)) * 2,
        act_r = AL_ACT + w * 128 + (lane & 1) * 64 + g * 16, s8_o = i16 < 2 ? WS_S8 + 5 * w * (108 * 128) + fj * 128 + i16 * 64 + g * 16 : OOB;
    int s6_o = lane < 40 ? WS_S6 + cp * S6_COPY + (bb * 640 + m0) * 4 : OOB;
    PIN(h2_w); PIN(h2_r); PIN(red_w); PIN(red_r); PIN(act_w); PIN(act_r); PIN(s8_o); PIN(s6_o);
    f32x4 at;
    _Pragma("nounroll") for (u32 sp = 0; sp < SPIN_CAP; ++sp) {
      CBAR();
      v4u t[8];
#pragma unroll
      for (int k = 0; k < 8; ++k) t[k] = wld128(ws, s6_o + k * 5120);
      at = ((asf4(t[0]) + asf4(t[1])) + (asf4(t[2]) + asf4(t[3]))) + ((asf4(t[4]) + asf4(t[5])) + (asf4(t[6]) + asf4(t[7])));
      u32 bad = 0;
#pragma unroll
      for (int k = 0; k < 8; ++k) bad |= tbad4(t[k], T);
      if (__all(lane >= 40 || bad == 0)) break;
      __builtin_amdgcn_s_sleep(S6_SLEEP);
    }
    const f32x4 x1 = xf + gav * at;
    STAMP(8);
    const bool act = lane < 40;
    const float pv = __builtin_bit_cast(float, __builtin_amdgcn_readfirstlane(__builtin_bit_cast(int, x1[0])));
    const float d0 = x1[0] - pv, d1 = x1[1] - pv, d2 = x1[2] - pv, d3 = x1[3] - pv;
    const float s1 = wave_sum(act ? (d0 + d1) + (d2 + d3) : 0.f);
    const float s2 = wave_sum(act ? (d0 * d0 + d1 * d1) + (d2 * d2 + d3 * d3) : 0.f);
    const float mw = pv + s1 * (1.f / 160.f), m2 = s2 - s1 * s1 * (1.f / 160.f);
    if (lane == 0) { LAS f32x2* sp = (LAS f32x2*)(L + AL_STAT); sp[w] = f32x2{mw, m2}; }
    BAR();
    const LAS f32x4* sp = (const LAS f32x4*)(L + AL_STAT + bb * 32);
    const f32x4 s01 = sp[0], s23 = sp[1];
    const float mean = ((s01[0] + s01[2]) + (s23[0] + s23[2])) * 0.25f;
    const float e0 = s01[0] - mean, e1 = s01[2] - mean, e2 = s23[0] - mean, e3 = s23[2] - mean;
    const float var = (((s01[1] + s01[3]) + (s23[1] + s23[3])) + 160.f * ((e0 * e0 + e1 * e1) + (e2 * e2 + e3 * e3))) * (1.f / 640.f);
    const float rstd = rsqrtf(var + 1e-5f);
    float hv[4];
#pragma unroll
    for (int k = 0; k < 4; ++k) hv[k] = (x1[k] - mean) * rstd * cb1[k] + sbv[k];
    if (act) { const v2u o = {pk_bf16(hv[0], hv[1]), pk_bf16(hv[2], hv[3])}; *(LAS v2u*)(L + h2_w) = o; }
    BAR();
    v4u bh[5];
#pragma unroll
    for (int i = 0; i < 5; ++i) bh[i] = ldsr128(L + h2_r + 256 * i);
    f32x4 a = Z4;
#pragma unroll
    for (int i = 0; i < 5; ++i) a = mfma16(ag[i], bh[i], a);
    if (i16 < 2) *(LAS f32x4*)(L + red_w) = a;
    BAR();
    const LAS float* wr = (const LAS float*)(L + red_r);
    const float gs = (wr[0] + wr[64]) + (wr[128] + wr[192]), us = (wr[32] + wr[96]) + (wr[160] + wr[224]);
    const float av = silu_f(gs) * us;
    *(LAS u16*)(L + act_w) = lane < 32 ? (u16)(pk_bf16(av, 0.f) & 0xFFFFu) : (u16)0;
    LGKM0();
    STAMP(9);
    const v4u bq = ldsr128(L + act_r);
    f32x4 acc[5];
#pragma unroll
    for (int k = 0; k < 5; ++k) acc[k] = mfma16(ad[k], bq, Z4);
#pragma unroll
    for (int k = 0; k < 5; ++k) wst128(ws, s8_o + k * (108 * 128), tag4(acc[k], T));
  }
  STAMP(10);
}

__global__ void __launch_bounds__(512, 2)
fused_decode(const u16* x, const u16* ckv, const u16* wq, const u16* wuk, const u16* wuv, const u16* wo, const u16* wg,
             const u16* wu, const u16* wd, const u16* c, const u16* wmod, u16* out, char* wsp) {
  extern __shared__ __attribute__((aligned(1024))) char lds[];
  LAS char* L = (LAS char*)lds;
  const int lane = threadIdx.x & 63, w = __builtin_amdgcn_readfirstlane(threadIdx.x >> 6);
  const rsrc_t ws = __builtin_amdgcn_make_buffer_rsrc((void*)wsp, 0, WS_END, 0x00020000);
  u64 pc;
  asm volatile("s_getpc_b64 %0" : "=s"(pc));
  Ptrs P{x, ckv, wq, wuk, wuv, wo, wg, wu, wd, c, wmod, out, wsp, (const char*)(pc & ~255ull), __builtin_amdgcn_s_memrealtime()};
  if (threadIdx.x == 0) { *(LAS u32*)(L + L_CNT) = 0u; *(LAS u32*)(L + AL_CNT) = 0u; }
  BAR();
  if (blockIdx.x < 128) f_role(P, L, ws, blockIdx.x, w, lane);
  else a_role(P, L, ws, blockIdx.x - 128, w, lane);

}

extern "C" void kernel_launch(void* const* d_in, const int* in_sizes, int n_in, void* d_out, int out_size, void* d_ws,
                              size_t ws_size, hipStream_t stream) {
  (void)in_sizes; (void)n_in; (void)out_size; (void)ws_size;
  (void)hipFuncSetAttribute((const void*)fused_decode, hipFuncAttributeMaxDynamicSharedMemorySize, LDS_BYTES);
  fused_decode<<<256, 512, LDS_BYTES, stream>>>((const u16*)d_in[0], (const u16*)d_in[1], (const u16*)d_in[2], (const u16*)d_in[3],
                                                (const u16*)d_in[4], (const u16*)d_in[5], (const u16*)d_in[7], (const u16*)d_in[8],
                                                (const u16*)d_in[9], (const u16*)d_in[10], (const u16*)d_in[11], (u16*)d_out, (char*)d_ws);
}
```

```cpp
#include <hip/hip_runtime.h>
#include <hip/hip_bf16.h>
#include <stdint.h>

typedef unsigned short u16;
typedef unsigned int u32;
typedef unsigned long long u64;
typedef u32 v4u __attribute__((ext_vector_type(4)));
typedef u32 v2u __attribute__((ext_vector_type(2)));
typedef float f32x4 __attribute__((ext_vector_type(4)));
typedef float f32x2 __attribute__((ext_vector_type(2)));
typedef float f32x16 __attribute__((ext_vector_type(16)));
typedef short s16x4 __attribute__((ext_vector_type(4)));
typedef short s16x8 __attribute__((ext_vector_type(8)));
typedef __bf16 bf16x8 __attribute__((ext_vector_type(8)));
typedef __bf16 bf16x2 __attribute__((ext_vector_type(2)));
#define GAS __attribute__((address_space(1)))
#define LAS __attribute__((address_space(3)))
#define DI __device__ __forceinline__

#define A_DELAY 0
#define F_TAIL_DELAY 700

#define S6_SLEEP 1
#define S4_SLEEP 8
#define CODE_PF 24576
constexpr u32 SENT = 0xFFFFFFFFu;
constexpr u32 SPIN_CAP = 1u << 17;

constexpr int WS_EPOCH = 0;
constexpr int WS_FLAGF = 256;
constexpr int S1_COPY = 5 * 4 * 640 * 4, S3_COPY = 2 * 32 * 512 * 2, S6_COPY = 8 * 2 * 640 * 4;
constexpr int WS_S1 = WS_FLAGF + 128 * 128 + 3840;
constexpr int WS_S2 = WS_S1 + 8 * S1_COPY;
constexpr int WS_S3 = WS_S2 + 32 * 4 * 1024;
constexpr int WS_S5 = WS_S3 + 8 * S3_COPY;
constexpr int WS_S6 = WS_S5 + 32 * 4 * 1024;
constexpr int WS_MB = WS_S6 + 8 * S6_COPY;
constexpr int WS_S8 = WS_MB + 4 * 2 * 640 * 4;
constexpr int WS_ST = WS_S8 + 40 * 108 * 32 * 4;
constexpr int WS_S4 = WS_ST + 128 * 8 * 128;
constexpr int WS_END = WS_S4 + 32 * 4 * 2 * 64 * 256;
constexpr int WS_STAMP = WS_END;
static_assert(WS_S1 % 256 == 0 && WS_S2 % 256 == 0 && WS_S3 % 256 == 0 && WS_S5 % 256 == 0 && WS_S6 % 256 == 0 && WS_MB % 256 == 0 &&
              WS_S8 % 256 == 0 && WS_ST % 256 == 0 && WS_S4 % 256 == 0, "align");

constexpr int LDS_BYTES = 163840;

typedef __amdgpu_buffer_rsrc_t rsrc_t;
DI v4u wld128(rsrc_t r, int off) { return __builtin_amdgcn_raw_buffer_load_b128(r, off, 0, 16); }
DI v2u wld64(rsrc_t r, int off) { return __builtin_amdgcn_raw_buffer_load_b64(r, off, 0, 16); }
DI u32 wld32(rsrc_t r, int off) { return __builtin_amdgcn_raw_buffer_load_b32(r, off, 0, 16); }
DI void wst128(rsrc_t r, int off, v4u v) { __builtin_amdgcn_raw_buffer_store_b128(v, r, off, 0, 16); }
DI void wst64(rsrc_t r, int off, v2u v) { __builtin_amdgcn_raw_buffer_store_b64(v, r, off, 0, 16); }
DI void wst32(rsrc_t r, int off, u32 v) { __builtin_amdgcn_raw_buffer_store_b32(v, r, off, 0, 16); }
DI u32 canon(u32 x) { return x < 0xFFFFFFFEu ? x : 0xFFFFFFFEu; }
DI bool ok4(v4u v) { return v.x != SENT && v.y != SENT && v.z != SENT && v.w != SENT; }
DI v4u tag4(f32x4 v, u32 T) {
  v4u o;
  o.x = __builtin_amdgcn_perm(__float_as_uint(v[0]), T, 0x07060500u); o.y = __builtin_amdgcn_perm(__float_as_uint(v[1]), T, 0x07060501u);
  o.z = __builtin_amdgcn_perm(__float_as_uint(v[2]), T, 0x07060502u); o.w = __builtin_amdgcn_perm(__float_as_uint(v[3]), T, 0x07060503u);
  return o;
}
DI bool tok4(v4u v, u32 T) { return (__builtin_amdgcn_perm(v.y, v.x, 0x0c0c0400u) | __builtin_amdgcn_perm(v.w, v.z, 0x04000c0cu)) == T; }
DI u32 tbad4(v4u v, u32 T) { return (__builtin_amdgcn_perm(v.y, v.x, 0x0c0c0400u) | __builtin_amdgcn_perm(v.w, v.z, 0x04000c0cu)) ^ T; }
constexpr int OOB = 0x40000000;
DI f32x4 asf4(v4u v) { const v4u m = v & 0xFFFFFF00u; return __builtin_bit_cast(f32x4, m); }
DI u32 mk_tag(u32 epoch_word) { return (epoch_word + 1u) * 0x01010101u; }
#define VM0() do { __builtin_amdgcn_s_waitcnt(0x0F70); asm volatile("" ::: "memory"); } while (0)
DI float bflo(u32 x) { return __uint_as_float(x << 16); }
DI float bfhi(u32 x) { return __uint_as_float(x & 0xffff0000u); }
DI u32 pk_bf16(float lo, float hi) { f32x2 f = {lo, hi}; bf16x2 h = __builtin_convertvector(f, bf16x2); return __builtin_bit_cast(u32, h); }
DI float silu_f(float x) { return x * __builtin_amdgcn_rcpf(1.f + __expf(-x)); }
DI v4u ldnt128(const void* p) { return __builtin_nontemporal_load((const GAS v4u*)p); }
DI f32x4 mfma16(v4u a, v4u b, f32x4 c) { return __builtin_amdgcn_mfma_f32_16x16x32_bf16(__builtin_bit_cast(bf16x8, a), __builtin_bit_cast(bf16x8, b), c, 0, 0, 0); }
DI f32x16 mfma32(v4u a, v4u b, f32x16 c) { return __builtin_amdgcn_mfma_f32_32x32x16_bf16(__builtin_bit_cast(bf16x8, a), __builtin_bit_cast(bf16x8, b), c, 0, 0, 0); }
#define BAR() asm volatile("s_waitcnt lgkmcnt(0)\n\ts_barrier" ::: "memory")
#define CBAR() asm volatile("" ::: "memory")
template <int N> DI void vmwait() { asm volatile("s_waitcnt vmcnt(%0)" :: "i"(N) : "memory"); }

template <int CTRL> DI float dpp(float x) { return __builtin_bit_cast(float, __builtin_amdgcn_mov_dpp(__builtin_bit_cast(int, x), CTRL, 0xf, 0xf, true)); }
DI float x16_sum(float x) { auto s = __builtin_amdgcn_permlane16_swap(__float_as_uint(x), __float_as_uint(x), false, false); return __uint_as_float(s[0]) + __uint_as_float(s[1]); }
DI float x32_sum(float x) { auto s = __builtin_amdgcn_permlane32_swap(__float_as_uint(x), __float_as_uint(x), false, false); return __uint_as_float(s[0]) + __uint_as_float(s[1]); }
DI float x16_max(float x) { auto s = __builtin_amdgcn_permlane16_swap(__float_as_uint(x), __float_as_uint(x), false, false); return fmaxf(__uint_as_float(s[0]), __uint_as_float(s[1])); }
DI float x32_max(float x) { auto s = __builtin_amdgcn_permlane32_swap(__float_as_uint(x), __float_as_uint(x), false, false); return fmaxf(__uint_as_float(s[0]), __uint_as_float(s[1])); }
DI float wave_sum(float x) { x += dpp<0xB1>(x); x += dpp<0x4E>(x); x += dpp<0x141>(x); x += dpp<0x140>(x); return x32_sum(x16_sum(x)); }

DI void dma_s(u32 ldsaddr, const void* sbase, u32 voff) {
  u32 keep;
  asm volatile("s_mov_b32 %0, m0\n\ts_mov_b32 m0, %1\n\ts_nop 0\n\tglobal_load_lds_dwordx4 %2, %3 nt\n\ts_mov_b32 m0, %0"
               : "=&s"(keep) : "s"(ldsaddr), "v"(voff), "s"(sbase) : "memory");
}
DI void dma_s_keep(u32 ldsaddr, const void* sbase, u32 voff) {
  u32 keep;
  asm volatile("s_mov_b32 %0, m0\n\ts_mov_b32 m0, %1\n\ts_nop 0\n\tglobal_load_lds_dwordx4 %2, %3\n\ts_mov_b32 m0, %0"
               : "=&s"(keep) : "s"(ldsaddr), "v"(voff), "s"(sbase) : "memory");
}
DI void dma_v(u32 ldsaddr, const void* vaddr) {
  u32 keep;
  asm volatile("s_mov_b32 %0, m0\n\ts_mov_b32 m0, %1\n\ts_nop 0\n\tglobal_load_lds_dwordx4 %2, off nt\n\ts_mov_b32 m0, %0"
               : "=&s"(keep) : "s"(ldsaddr), "v"(vaddr) : "memory");
}
DI u32 swz(u32 row) { return ((row & 3u) << 2) | ((row >> 2) & 3u); }
DI v4u ldsr128(const LAS char* p) { return *(const LAS v4u*)p; }
DI v4u tr2(const LAS char* p0, const LAS char* p1) {
  s16x4 a = __builtin_amdgcn_ds_read_tr16_b64_v4i16((LAS s16x4*)p0);
  s16x4 b = __builtin_amdgcn_ds_read_tr16_b64_v4i16((LAS s16x4*)p1);
  s16x8 v = __builtin_shufflevector(a, b, 0, 1, 2, 3, 4, 5, 6, 7);
  return __builtin_bit_cast(v4u, v);
}
DI void wait_ticks(u64 t0, u32 ticks) { if (ticks) { while (__builtin_amdgcn_s_memrealtime() - t0 < ticks) __builtin_amdgcn_s_sleep(2); } }

#define STAMP(i) do { } while (0)
#define STARG

struct Ptrs { const u16 *x, *ckv, *wq, *wuk, *wuv, *wo, *wg, *wu, *wd, *c, *wmod; u16* out; char* ws; const char* code; u64 t0; };

DI v4u frag64(const LAS char* img, int lane, int c, int ks) {
  const int g = lane >> 4, q = (lane & 15) >> 2, p = lane & 3;
  const LAS char* a = img + (32 * ks + 8 * g + q) * 64 + 32 * c + 8 * p;
  return tr2(a, a + 4 * 64);
}
DI v4u frag256(const LAS char* img, int lane, int c, int ks) {
  const int g = lane >> 4, q = (lane & 15) >> 2, p = lane & 3;
  const u32 r0 = 8 * g + q, r1 = r0 + 4, ch = 2 * c + (p >> 1);
  return tr2(img + 8192 * ks + 256 * r0 + 16 * (ch ^ swz(r0)) + 8 * (p & 1), img + 8192 * ks + 256 * r1 + 16 * (ch ^ swz(r1)) + 8 * (p & 1));
}
template <int N> DI f32x4 gather_sum(rsrc_t ws, int off, int stride, u32 T) {
  f32x4 s;
  _Pragma("nounroll") for (u32 sp = 0; sp < SPIN_CAP; ++sp) {
    CBAR();
    v4u t[N];
#pragma unroll
    for (int k = 0; k < N; ++k) t[k] = wld128(ws, off + k * stride);
    u32 bad = 0;
#pragma unroll
    for (int k = 0; k < N; ++k) bad |= tbad4(t[k], T);
    s = asf4(t[0]);
#pragma unroll
    for (int k = 1; k < N; ++k) s += asf4(t[k]);
    if (__all(bad == 0)) break;
  }
  return s;
}
template <int SLEEP> DI void wait_piece(rsrc_t ws, int off, u32 T, int lane) {
  _Pragma("nounroll") for (u32 sp = 0; sp < SPIN_CAP; ++sp) {
    CBAR();
    const v4u f = wld128(ws, lane == 0 ? off : OOB);
    if (__builtin_amdgcn_readfirstlane(tbad4(f, T)) == 0) break;
    __builtin_amdgcn_s_sleep(SLEEP);
  }
}
DI u32 lds_ld(const LAS char* p) { return *(const volatile LAS u32*)p; }
#define PIN(x) asm volatile("" : "+v"(x))
DI void lds_wait_ge(const LAS char* p, u32 n) { _Pragma("nounroll") for (u32 sp = 0; sp < (1u << 22); ++sp) { if ((u32)__builtin_amdgcn_readfirstlane(lds_ld(p)) >= n) break; } CBAR(); }
DI void lds_arrive(LAS char* p, int lane) { asm volatile("s_waitcnt lgkmcnt(0)" ::: "memory"); if (lane == 0) __hip_atomic_fetch_add((LAS u32*)p, 1u, __ATOMIC_RELAXED, __HIP_MEMORY_SCOPE_WORKGROUP); }
#define LGKM0() asm volatile("s_waitcnt lgkmcnt(0)" ::: "memory")

constexpr int L_CNT = LDS_BYTES - 64;
constexpr int FL_WQ = 0, FL_WUK = 40960, FL_WO = 0, FL_WUV = 81920, FL_SM = FL_WUV + 32768;
constexpr int FL_HB = FL_SM, FL_QB = FL_HB + 1024, FL_QA = FL_QB + 512, FL_CRED = FL_QA + 512, FL_CST = FL_CRED + 4096,
              FL_CB = FL_CST + 64, FL_CTX = FL_CB + 512, FL_WRED = FL_CTX + 4096, FL_FIN = FL_WRED + 4096, FL_PF = FL_FIN + 1024,
              FL_SB = FL_PF + 1024  ;
static_assert(FL_PF % 16 == 0 && FL_SB + 2560 <= L_CNT, "lds");

DI void f_role(const Ptrs& P, LAS char* L, rsrc_t ws, int p, int w, int lane STARG) {
  const int i16 = lane & 15, g = lane >> 4;
  const int h = p & 31, sl = p >> 5, cp = p & 7;
  const f32x4 Z4 = {0.f, 0.f, 0.f, 0.f};
  float ln[4] = {0.f, 0.f, 0.f, 0.f};
  u32 T = 0;
  if (w < 2) {
    T = wld32(ws, WS_EPOCH);
    {
      const v4u S = {SENT, SENT, SENT, SENT};
#pragma unroll
      for (int k = 0; k < 2; ++k) wst128(ws, WS_S3 + (4 * w + 2 * k + (lane >> 5)) * S3_COPY + (((lane >> 4) & 1) * 32 + h) * 1024 + sl * 256 + i16 * 16, S);
    }
    const v4u xa = *(const GAS v4u*)(P.x + w * 640 + 8 * lane);
    const v4u xb = *(const GAS v4u*)(P.x + w * 640 + 512 + 8 * (lane & 15));
    const v2u xs = *(const GAS v2u*)(P.x + w * 640 + 160 * sl + 4 * (lane < 40 ? lane : 0));
    VM0();
    T = __builtin_amdgcn_readfirstlane(mk_tag(T));
    { const v2u f = {T, ~T}; wst64(ws, lane == 0 ? WS_FLAGF + p * 128 + 8 * w : OOB, f); }
    if (w == 0 && lane == 0) *(LAS u32*)(L + L_CNT + 4) = T;
    float v[16];
#pragma unroll
    for (int k = 0; k < 4; ++k) { v[2 * k] = bflo(xa[k]); v[2 * k + 1] = bfhi(xa[k]); v[8 + 2 * k] = bflo(xb[k]); v[9 + 2 * k] = bfhi(xb[k]); }
    const bool ex = lane < 16;
    float s = 0.f;
#pragma unroll
    for (int k = 0; k < 8; ++k) s += v[k] + (ex ? v[8 + k] : 0.f);
    const float mean = wave_sum(s) * (1.f / 640.f);
    float s2 = 0.f;
#pragma unroll
    for (int k = 0; k < 8; ++k) { const float d = v[k] - mean, e = v[8 + k] - mean; s2 += d * d + (ex ? e * e : 0.f); }
    const float rstd = rsqrtf(wave_sum(s2) * (1.f / 640.f) + 1e-5f);
    ln[0] = (bflo(xs.x) - mean) * rstd; ln[1] = (bfhi(xs.x) - mean) * rstd; ln[2] = (bflo(xs.y) - mean) * rstd; ln[3] = (bfhi(xs.y) - mean) * rstd;
  } else if (w < 6) {
    const char* sb = (const char*)P.wq + (size_t)(160 * sl) * 8192 + h * 256;
    const u32 n0 = w - 2;
    const u32 vo = g * 8192 + ((i16 ^ ((g << 2) | n0)) << 4);
#pragma unroll 2
    for (int k = 0; k < 10; ++k) dma_s(FL_WQ + (n0 + 4 * k) * 1024, sb + (size_t)(n0 + 4 * k) * 4 * 8192, vo);
    vmwait<0>();
    lds_arrive(L + L_CNT, lane);
  } else {
    __builtin_amdgcn_s_sleep(2);
    const char* su = (const char*)P.wuk + (size_t)(128 * sl) * 8192 + h * 256;
    const u32 n0 = w - 6;
    const u32 vo0 = g * 8192 + ((i16 ^ ((g << 2) | n0)) << 4), vo1 = g * 8192 + ((i16 ^ ((g << 2) | (n0 + 2))) << 4);
#pragma unroll 1
    for (int k = 0; k < 8; ++k) {
      dma_s(FL_WUK + (n0 + 4 * k) * 1024, su + (size_t)(n0 + 4 * k) * 4 * 8192, vo0);
      dma_s(FL_WUK + (n0 + 4 * k + 2) * 1024, su + (size_t)(n0 + 4 * k + 2) * 4 * 8192, vo1);
    }
    if (w == 7) {
      const int o0 = (p >> 3) * 1024;
      dma_s_keep(FL_PF, P.code + (o0 < CODE_PF - 1024 ? o0 : CODE_PF - 1024), lane * 16);
      dma_s_keep(FL_PF, P.code + (o0 + 16384 < CODE_PF - 1024 ? o0 + 16384 : CODE_PF - 1024), lane * 16);
    }
  }
  STAMP(1);
  u32 hb_a = FL_HB + (lane & 1) * 320 + g * 16, s2_o = i16 < 2 ? WS_S2 + (h * 4 + sl) * 1024 + w * 128 + i16 * 64 + g * 16 : OOB;
  PIN(hb_a); PIN(s2_o);
  lds_wait_ge(L + L_CNT, 4u);
  v4u aq[5];
#pragma unroll
  for (int k = 0; k < 5; ++k) aq[k] = frag256(L + FL_WQ, lane, w, k);
  STAMP(2);
  if (w < 2) {
    f32x4 sa, ca;
    const int s1_o = lane < 40 ? WS_S1 + cp * S1_COPY + (w * 640 + 160 * sl + 4 * lane) * 4 : OOB;
    _Pragma("nounroll") for (u32 sp = 0; sp < SPIN_CAP; ++sp) {
      CBAR();
      v4u t[10];
#pragma unroll
      for (int k = 0; k < 10; ++k) t[k] = wld128(ws, s1_o + ((k >> 1) * 4 + (k & 1) * 2) * 2560);
      u32 bad = 0;
#pragma unroll
      for (int k = 0; k < 10; ++k) bad |= tbad4(t[k], T);
      sa = asf4(t[0]); ca = asf4(t[1]);
#pragma unroll
      for (int k = 1; k < 5; ++k) { sa += asf4(t[2 * k]); ca += asf4(t[2 * k + 1]); }
      if (__all(lane >= 40 || bad == 0)) break;
    }
    const float h0 = ln[0] * (1.f + ca[0]) + sa[0], h1 = ln[1] * (1.f + ca[1]) + sa[1];
    const float h2 = ln[2] * (1.f + ca[2]) + sa[2], h3 = ln[3] * (1.f + ca[3]) + sa[3];
    if (lane < 40) { const v2u o = {pk_bf16(h0, h1), pk_bf16(h2, h3)}; *(LAS v2u*)(L + FL_HB + w * 320 + lane * 8) = o; }
    STAMP(4);
  }
  BAR();
  {
    v4u bh[5];
#pragma unroll
    for (int k = 0; k < 5; ++k) bh[k] = ldsr128(L + hb_a + 64 * k);
    T = __builtin_amdgcn_readfirstlane(lds_ld(L + L_CNT + 4));
    f32x4 acc = Z4;
#pragma unroll
    for (int k = 0; k < 5; ++k) acc = mfma16(aq[k], bh[k], acc);
    wst128(ws, s2_o, tag4(acc, T));
  }
  STAMP(5);
  u32 qb_a = FL_QB + (lane & 1) * 256 + g * 16, uk_a[4], qa_w = FL_QA + i16 * 256 + (16 * w + 4 * g) * 2;
  {
    const u32 row = 16 * w + i16, sw = swz(row);
#pragma unroll
    for (int k = 0; k < 4; ++k) { uk_a[k] = FL_WUK + row * 256 + (((4 * k + g) ^ sw) << 4); PIN(uk_a[k]); }
  }
  PIN(qb_a); PIN(qa_w);
  if (w == 0) {
    const f32x4 q = gather_sum<4>(ws, WS_S2 + h * 4096 + lane * 16, 1024, T);
    const v2u o = {pk_bf16(q[0], q[1]), pk_bf16(q[2], q[3])};
    *(LAS v2u*)(L + FL_QB + ((lane >> 2) & 1) * 256 + (16 * (lane >> 3) + 4 * (lane & 3)) * 2) = o;
    STAMP(6);
  } else if (w >= 6) {
    vmwait<0>();
  }
  BAR();
  {
    v4u bq[4], au[4];
#pragma unroll
    for (int k = 0; k < 4; ++k) { au[k] = ldsr128(L + uk_a[k]); bq[k] = ldsr128(L + qb_a + 64 * k); }
    f32x4 acc = Z4;
#pragma unroll
    for (int k = 0; k < 4; ++k) acc = mfma16(au[k], bq[k], acc);
    constexpr float SC = 0.08838834764831845f * 1.4426950408889634f;
    if (i16 < 2) { const v2u o = {canon(pk_bf16(acc[0] * SC, acc[1] * SC)), canon(pk_bf16(acc[2] * SC, acc[3] * SC))};
      *(LAS v2u*)(L + qa_w) = o; }
  }
  BAR();
  wst128(ws, lane < 32 ? WS_S3 + w * S3_COPY + ((lane >> 4) * 32 + h) * 1024 + sl * 256 + i16 * 16 : OOB, ldsr128(L + FL_QA + (lane & 31) * 16));
  STAMP(7);
  const int kq = p / 10, ms = p % 10;
  if (w >= 4) {
    if (p < 88) wait_ticks(P.t0, F_TAIL_DELAY);
    const u32 n0 = w - 4;
    const char* sb = (const char*)P.wuv + (size_t)(128 * sl) * 8192 + h * 256;
    const u32 vo = g * 8192 + ((i16 ^ ((g << 2) | n0)) << 4);
#pragma unroll 2
    for (int k = 0; k < 8; ++k) dma_s(FL_WUV + (n0 + 4 * k) * 1024, sb + (size_t)(n0 + 4 * k) * 4 * 8192, vo);
    if (p < 80) {
      const char* so = (const char*)P.wo + (size_t)(512 * kq) * 1280 + ms * 128;
      const u32 vo2 = (lane >> 3) * 1280 + (lane & 7) * 16;
#pragma unroll 2
      for (int n = n0; n < 64; n += 4) dma_s(FL_WO + n * 1024, so + (size_t)n * 8 * 1280, vo2);
    } else if (p >= 88) {
      const char* sm = (const char*)P.wmod + (size_t)(1280 + 64 * (p - 88)) * 2;
      const u32 vo2 = (lane >> 3) * 7680 + (lane & 7) * 16;
#pragma unroll 2
      for (int n = n0; n < 80; n += 4) dma_s(FL_WO + n * 1024, sm + (size_t)n * 8 * 7680, vo2);
    }
    vmwait<0>();
  } else if (p >= 88 && w < 3) {
    const int tid = w * 64 + lane;
    const v4u cvv = *(const GAS v4u*)(P.c + (tid < 160 ? tid : 0) * 8);
    v4u sv;
#pragma unroll
    for (int k = 0; k < 4; ++k) sv[k] = pk_bf16(silu_f(bflo(cvv[k])), silu_f(bfhi(cvv[k])));
    if (tid < 160) *(LAS v4u*)(L + FL_SB + tid * 16) = sv;
  }
  BAR();
  STAMP(8);
  if (p >= 88) {
    f32x4 a[4] = {Z4, Z4, Z4, Z4};
    const int q = (lane & 15) >> 2, pp = lane & 3;
    for (int ks = w; ks < 20; ks += 8) {
      const v4u b = ldsr128(L + FL_SB + (lane & 1) * 1280 + (32 * ks + 8 * g) * 2);
      const LAS char* base = L + FL_WO + (32 * ks + 8 * g + q) * 128 + 8 * pp;
#pragma unroll
      for (int c = 0; c < 4; ++c) a[c] = mfma16(tr2(base + 32 * c, base + 32 * c + 4 * 128), b, a[c]);
    }
    if (i16 < 2) {
      LAS f32x4* wr = (LAS f32x4*)(L + FL_WRED + w * 512 + i16 * 256 + g * 16);
#pragma unroll
      for (int c = 0; c < 4; ++c) wr[4 * c] = a[c];
    }
    BAR();
    if (w == 0) {
      const LAS f32x4* wr = (const LAS f32x4*)(L + FL_WRED + (lane & 31) * 16);
      f32x4 v = wr[0];
#pragma unroll
      for (int k = 1; k < 8; ++k) v += wr[32 * k];
      const int col = 64 * (p - 88);
      wst128(ws, lane < 32 ? WS_MB + (((col / 640) * 2 + (lane >> 4)) * 640 + col % 640) * 4 + (lane & 15) * 16 : OOB, tag4(v, T));
    }
  }
  v4u uv[4], ao[8];
#pragma unroll
  for (int k = 0; k < 4; ++k) uv[k] = frag256(L + FL_WUV, lane, w, k);
  if (p < 80) {
    const LAS char* base = L + FL_WO + (64 * w + 8 * g + ((lane & 15) >> 2)) * 128 + 8 * (lane & 3);
#pragma unroll
    for (int k = 0; k < 8; ++k) { const LAS char* a = base + (k >> 2) * 32 * 128 + (k & 3) * 32; ao[k] = tr2(a, a + 4 * 128); }
  }
  {
    const int b = w >> 2, k0 = 16 * (w & 3);
    const int sto = WS_ST + ((b * 64 + k0 + i16) * 8 + (h & 7)) * 128 + (h >> 4) * 16;
    int s4_o = WS_S4 + ((((h * 4 + sl) * 2 + b) * 64 + k0) * 256) + lane * 16;
    u32 cb_a = FL_CB + (lane & 1) * 256 + g * 16, s5_o = i16 < 2 ? WS_S5 + (h * 4 + sl) * 1024 + w * 128 + i16 * 64 + g * 16 : OOB, cr_a = FL_CRED + w * 512 + (lane & 15) * 32;
    PIN(s4_o); PIN(cb_a); PIN(s5_o); PIN(cr_a);
    if (w == 0) wait_piece<8>(ws, sto, T, lane);
    BAR();
    v4u s;
    _Pragma("nounroll") for (u32 sp = 0; sp < SPIN_CAP; ++sp) {
      CBAR();
      s = wld128(ws, lane < 16 ? sto : OOB);
      if (__all(lane >= 16 || tbad4(s, T) == 0)) break;
      __builtin_amdgcn_s_sleep(2);
    }
    float wj[4];
    {
      const bool odd = (h >> 3) & 1;
      const float m = __uint_as_float((odd ? s.z : s.x) & 0xFFFFFF00u), l = __uint_as_float((odd ? s.w : s.y) & 0xFFFFFF00u);
      float Mw = fmaxf(m, dpp<0xB1>(m)); Mw = fmaxf(Mw, dpp<0x4E>(Mw)); Mw = fmaxf(Mw, dpp<0x141>(Mw)); Mw = fmaxf(Mw, dpp<0x140>(Mw));
      const float e = __builtin_amdgcn_exp2f(m - Mw);
      float Lw = e * l; Lw += dpp<0xB1>(Lw); Lw += dpp<0x4E>(Lw); Lw += dpp<0x141>(Lw); Lw += dpp<0x140>(Lw);
      if (lane == 0) *(LAS f32x2*)(L + FL_CST + w * 8) = f32x2{Mw, Lw};
#pragma unroll
      for (int j = 0; j < 4; ++j) wj[j] = __shfl(e, 4 * j + g);
    }
    STAMP(9);
    __builtin_amdgcn_s_sleep(S4_SLEEP);
    v4u t[4];
    _Pragma("nounroll") for (u32 sp = 0; sp < SPIN_CAP; ++sp) {
      CBAR();
#pragma unroll
      for (int j = 0; j < 4; ++j) t[j] = wld128(ws, s4_o + j * 1024);
      u32 mx = 0;
#pragma unroll
      for (int j = 0; j < 4; ++j) mx = max(mx, max(max(t[j].x, t[j].y), max(t[j].z, t[j].w)));
      if (__all(mx != SENT)) break;
    }
    STAMP(3);
    float acc[8];
#pragma unroll
    for (int k = 0; k < 4; ++k) { acc[2 * k] = wj[0] * bflo(t[0][k]); acc[2 * k + 1] = wj[0] * bfhi(t[0][k]); }
#pragma unroll
    for (int j = 1; j < 4; ++j) {
#pragma unroll
      for (int k = 0; k < 4; ++k) { acc[2 * k] += wj[j] * bflo(t[j][k]); acc[2 * k + 1] += wj[j] * bfhi(t[j][k]); }
    }
#pragma unroll
    for (int k = 0; k < 8; ++k) acc[k] = x32_sum(x16_sum(acc[k]));
    if (lane < 16) {
      LAS f32x4* cr = (LAS f32x4*)(L + cr_a);
      cr[0] = f32x4{acc[0], acc[1], acc[2], acc[3]}; cr[1] = f32x4{acc[4], acc[5], acc[6], acc[7]};
    }
    BAR();
    if (w == 0) {
      const int bb = lane >> 5;
      const LAS f32x4* cs = (const LAS f32x4*)(L + FL_CST + bb * 32);
      const f32x4 c01 = cs[0], c23 = cs[1];
      const LAS f32x4* cr = (const LAS f32x4*)(L + FL_CRED + bb * 2048 + (lane & 31) * 16);
      const f32x4 r0 = cr[0], r1 = cr[32], r2 = cr[64], r3 = cr[96];
      const float M = fmaxf(fmaxf(c01[0], c01[2]), fmaxf(c23[0], c23[2]));
      const float f0 = __builtin_amdgcn_exp2f(c01[0] - M), f1 = __builtin_amdgcn_exp2f(c01[2] - M), f2 = __builtin_amdgcn_exp2f(c23[0] - M), f3 = __builtin_amdgcn_exp2f(c23[2] - M);
      const float inv = __builtin_amdgcn_rcpf((f0 * c01[1] + f1 * c01[3]) + (f2 * c23[1] + f3 * c23[3]));
      const f32x4 v = ((r0 * f0 + r1 * f1) + (r2 * f2 + r3 * f3)) * inv;
      const v2u o = {pk_bf16(v[0], v[1]), pk_bf16(v[2], v[3])};
      *(LAS v2u*)(L + FL_CB + lane * 8) = o;
    }
    BAR();
    v4u bc[4];
#pragma unroll
    for (int k = 0; k < 4; ++k) bc[k] = ldsr128(L + cb_a + 64 * k);
    f32x4 a2 = Z4;
#pragma unroll
    for (int k = 0; k < 4; ++k) a2 = mfma16(uv[k], bc[k], a2);
    wst128(ws, s5_o, tag4(a2, T));
  }
  STAMP(10);
  if (p < 80) {
    u32 cx_w = FL_CTX + w * 256 + ((lane >> 2) & 1) * 128 + (16 * ((lane & 31) >> 3) + 4 * (lane & 3)) * 2, cx_r = FL_CTX + w * 256 + (lane & 1) * 128 + g * 16,
        wr_w = FL_WRED + w * 512 + (i16 & 1) * 256 + g * 16, wr_r = FL_WRED + (lane & 31) * 16,
        s6_o = lane < 32 ? WS_S6 + w * S6_COPY + ((kq * 2 + (lane >> 4)) * 640 + 64 * ms) * 4 + (lane & 15) * 16 : OOB;
    PIN(cx_w); PIN(cx_r); PIN(wr_w); PIN(wr_r); PIN(s6_o);
    f32x4 cx = gather_sum<2>(ws, WS_S5 + (4 * kq + (w >> 1)) * 4096 + (lane >> 5) * 1024 + (w & 1) * 512 + (lane & 31) * 16, 2048, T);
    STAMP(11);
#pragma unroll
    for (int k = 0; k < 4; ++k) cx[k] = x32_sum(cx[k]);
    const v2u o = {pk_bf16(cx[0], cx[1]), pk_bf16(cx[2], cx[3])};
    if (lane < 32) *(LAS v2u*)(L + cx_w) = o;
    LGKM0();
    v4u bx[2];
#pragma unroll
    for (int k = 0; k < 2; ++k) bx[k] = ldsr128(L + cx_r + 64 * k);
    f32x4 a[4];
#pragma unroll
    for (int c = 0; c < 4; ++c) a[c] = mfma16(ao[4 + c], bx[1], mfma16(ao[c], bx[0], Z4));
    if (i16 < 2) {
      LAS f32x4* wr = (LAS f32x4*)(L + wr_w);
#pragma unroll
      for (int c = 0; c < 4; ++c) wr[4 * c] = a[c];
    }
    BAR();
    {
      const LAS f32x4* wr = (const LAS f32x4*)(L + wr_r);
      f32x4 v[8];
#pragma unroll
      for (int k = 0; k < 8; ++k) v[k] = wr[32 * k];
      const f32x4 r = ((v[0] + v[1]) + (v[2] + v[3])) + ((v[4] + v[5]) + (v[6] + v[7]));
      wst128(ws, s6_o, tag4(r, T));
    }
    STAMP(12);
  } else if (p >= 88) {
    const int fs = p - 88, bm = lane & 7, bb = bm >> 2, m0 = 16 * fs + 4 * (bm & 3);
    f32x4 x1, gb;
    if (w == 0) {
      const v2u xv = *(const GAS v2u*)(P.x + bb * 640 + m0);
      _Pragma("nounroll") for (u32 sp = 0; sp < SPIN_CAP; ++sp) {
        CBAR();
        const v4u t0 = wld128(ws, WS_S6 + cp * S6_COPY + (((lane >> 3) * 2 + bb) * 640 + m0) * 4);
        const v4u t4 = wld128(ws, lane < 8 ? WS_MB + ((0 * 2 + bb) * 640 + m0) * 4 : OOB);
        const v4u t5 = wld128(ws, lane < 8 ? WS_MB + ((3 * 2 + bb) * 640 + m0) * 4 : OOB);
        const u32 bad = tbad4(t0, T) | (lane < 8 ? tbad4(t4, T) | tbad4(t5, T) : 0u);
        f32x4 at = asf4(t0);
#pragma unroll
        for (int k = 0; k < 4; ++k) { float v = at[k]; v += dpp<0x128>(v); at[k] = x32_sum(x16_sum(v)); }
        const f32x4 xf = {bflo(xv.x), bfhi(xv.x), bflo(xv.y), bfhi(xv.y)};
        x1 = xf + asf4(t4) * at;
        gb = asf4(t5);
        if (__all(bad == 0)) break;
        __builtin_amdgcn_s_sleep(4);
      }
    }
    BAR();
    STAMP(11);
    f32x4 y;
    const int tid = w * 64 + lane;
    _Pragma("nounroll") for (u32 sp = 0; sp < SPIN_CAP; ++sp) {
      CBAR();
      const v4u t0 = wld128(ws, WS_S8 + fs * (108 * 128) + tid * 16);
      const v4u t1 = wld128(ws, WS_S8 + fs * (108 * 128) + (tid < 352 ? 512 + tid : tid) * 16);
      y = asf4(t0);
      if (tid < 352) y += asf4(t1);
      if (__all((tbad4(t0, T) | tbad4(t1, T)) == 0)) break;
      __builtin_amdgcn_s_sleep(1);
    }
    STAMP(12);
#pragma unroll
    for (int k = 0; k < 4; ++k) { float v = y[k]; v += dpp<0x128>(v); y[k] = x32_sum(x16_sum(v)); }
    if (lane < 8) *(LAS f32x4*)(L + FL_FIN + w * 128 + lane * 16) = y;
    BAR();
    if (w == 0) {
      const LAS f32x4* fr = (const LAS f32x4*)(L + FL_FIN + bm * 16);
      f32x4 v[8];
#pragma unroll
      for (int k = 0; k < 8; ++k) v[k] = fr[8 * k];
      const f32x4 o = x1 + gb * (((v[0] + v[1]) + (v[2] + v[3])) + ((v[4] + v[5]) + (v[6] + v[7])));
      const v2u ob = {pk_bf16(o[0], o[1]), pk_bf16(o[2], o[3])};
      if (lane < 8) *(GAS v2u*)(P.out + bb * 640 + m0) = ob;
      if (p == 88 && lane == 0) wst32(ws, WS_EPOCH, wld32(ws, WS_EPOCH) + 1u);
    }
    STAMP(13);
  }
}

constexpr int AL_CKV = 0, AL_Q = 131072;
constexpr int AL_P = AL_Q, AL_MAX = AL_Q + 8192, AL_SUM = AL_MAX + 512;
constexpr int AL_O = 0, AL_GU = 32768, AL_DN = AL_Q + 10240  , AL_SM = AL_GU + 40960;
constexpr int AL_H2 = AL_SM, AL_STAT = AL_H2 + 2560, AL_RED = AL_STAT + 64, AL_ACT = AL_RED + 1024;
constexpr int AL_CNT = AL_Q + 4096;
static_assert(AL_ACT + 8 * 128 <= AL_Q && AL_SUM + 512 <= AL_DN && AL_DN + 20480 <= LDS_BYTES, "lds");

DI void a_role(const Ptrs& P, LAS char* L, rsrc_t ws, int j, int w, int lane STARG) {
  const int i16 = lane & 15, g = lane >> 4, l32 = lane & 31, hh = lane >> 5;
  const int b = j >> 6, kc = j & 63, cp = j & 7;
  const f32x4 Z4 = {0.f, 0.f, 0.f, 0.f};
  f32x16 S;
  const int fl = (j & 7) + 8 * (j >> 5), fsub = (j >> 3) & 3, fj = 4 * fl + fsub;
  const bool do_ffn = fl < 27;
  v4u dummy = {0u, 0u, 0u, 0u};
  u32 T = 0;
  u32 ca[8], qa[8];
  T = wld32(ws, WS_EPOCH);
  {
    const int un = (j & 7) + 8 * (j >> 4), cg = 2 * (un / 5) + ((j >> 3) & 1), ks1 = un % 5;
    if (un < 50) {
      const int base = 128 * ks1 + 16 * w;
      const int bb = (lane >> 3) & 1, r1 = i16 >> 3, mat = (i16 >> 2) & 1, ch = i16 & 3, jj = lane & 7;
      u32 cv[2]; v4u wm[2];
#pragma unroll
      for (int t = 0; t < 2; ++t) cv[t] = *(const GAS u32*)(P.c + bb * 640 + base + 8 * t + 2 * g);
#pragma unroll
      for (int t = 0; t < 2; ++t) wm[t] = ldnt128(P.wmod + (size_t)(base + 8 * t + 2 * g + r1) * 3840 + mat * 640 + 32 * cg + 8 * ch);
      u32 msk[4];
#pragma unroll
      for (int r = 0; r < 4; ++r) msk[r] = (r == (jj >> 1)) ? (0xFFFFu << (16 * (jj & 1))) : 0u;
      int s1_o = WS_S1 + w * S1_COPY + ((ks1 * 4 + (l32 >> 3)) * 640 + 32 * cg) * 4 + (lane & 7) * 16;
      if (lane >= 32) s1_o = OOB;
      PIN(s1_o);
      f32x4 a0 = Z4, a1 = Z4;
#pragma unroll
      for (int t = 0; t < 2; ++t) {
        const u32 sp = pk_bf16(silu_f(bflo(cv[t])), silu_f(bfhi(cv[t])));
        const u32 d0 = (sp & 0xFFFFu) * 0x10001u, d1 = (sp >> 16) * 0x10001u;
        const v4u B0 = {d0 & msk[0], d0 & msk[1], d0 & msk[2], d0 & msk[3]};
        const v4u B1 = {d1 & msk[0], d1 & msk[1], d1 & msk[2], d1 & msk[3]};
        a0 = mfma16(wm[t], B0, a0);
        a1 = mfma16(wm[t], B1, a1);
      }
      LAS float* red = (LAS float*)(L + AL_Q) + w * 128;
      const int mt = (lane >> 4) & 1;
#pragma unroll
      for (int r = 0; r < 4; ++r) {
        const float v = x32_sum(lane < 32 ? a0[r] : a1[r]);
        if (lane < 32) red[(mt * 2 + bb) * 32 + 8 * r + jj] = v;
      }
      T = __builtin_amdgcn_readfirstlane(mk_tag(T));
      BAR();
      {
        const LAS f32x4* rd = (const LAS f32x4*)(L + AL_Q) + l32;
        const f32x4 s = ((rd[0] + rd[32]) + (rd[64] + rd[96])) + ((rd[128] + rd[160]) + (rd[192] + rd[224]));
        wst128(ws, s1_o, tag4(s, T));
      }
      BAR();
    } else {
      T = __builtin_amdgcn_readfirstlane(mk_tag(T));
    }
  }
  if (j >= 112 && j < 120 && w == 3) { __builtin_amdgcn_fence(__ATOMIC_RELEASE, "agent"); asm volatile("s_waitcnt vmcnt(0)" ::: "memory"); }
  STAMP(14);
  if (w >= 4) {
    const int lw = w - 4;
    const char* sb = (const char*)P.ckv + ((size_t)(b * 8192 + kc * 128 + 32 * lw)) * 1024;
    wait_ticks(P.t0, A_DELAY);
#pragma unroll 1
    for (int i4 = 0; i4 < 2; ++i4) {
#pragma unroll
      for (int i = 0; i < 16; ++i) {
        const u32 sw = ((i & 3) << 2) | ((i >> 2) & 3);
        dma_s(AL_CKV + (32 * lw + 16 * i4 + i) * 1024, sb + (16 * i4 + i) * 1024, g * 256 + ((i16 ^ sw) << 4));
      }
    }
    STAMP(1);
    vmwait<0>();
  } else {
    {
      const v4u Sv = {SENT, SENT, SENT, SENT};
#pragma unroll
      for (int k = 0; k < 8; ++k) wst128(ws, WS_S4 + (((((8 * w + k) * 4 + g) * 2 + b) * 64 + kc) * 256) + i16 * 16, Sv);
    }
    {
      const u32 row = 32 * w + l32, sw = swz(row);
#pragma unroll
      for (int s = 0; s < 8; ++s) { ca[s] = AL_CKV + row * 1024 + (((2 * s + hh) ^ sw) << 4); qa[s] = AL_Q + l32 * 1024 + (((2 * s + hh) ^ (l32 & 15)) << 4); PIN(ca[s]); PIN(qa[s]); }
    }
    _Pragma("nounroll") for (u32 sp = 0; sp < SPIN_CAP; ++sp) {
      CBAR();
      const v4u f = wld128(ws, lane < 32 ? WS_FLAGF + (8 * w + (lane & 7) + 32 * (lane >> 3)) * 128 : OOB);
      if (__all(lane >= 32 || ((f.x ^ T) | (f.y ^ ~T) | (f.z ^ T) | (f.w ^ ~T)) == 0)) break;
      __builtin_amdgcn_s_sleep(8);
    }
    STAMP(1);
    const int s3_o = WS_S3 + cp * S3_COPY + (b * 32 + 8 * w) * 1024;
    _Pragma("nounroll") for (u32 sp = 0; sp < SPIN_CAP; ++sp) {
      CBAR();
      const u32 f = wld32(ws, lane == 0 ? s3_o : OOB);
      if (__builtin_amdgcn_readfirstlane(f) != SENT) break;
      __builtin_amdgcn_s_sleep(4);
    }
    _Pragma("nounroll") for (u32 sp = 0; sp < SPIN_CAP; ++sp) {
      CBAR();
      v4u t[8];
#pragma unroll
      for (int k = 0; k < 8; ++k) t[k] = wld128(ws, s3_o + k * 1024 + lane * 16);
      u32 mx = 0;
#pragma unroll
      for (int k = 0; k < 8; ++k) { const u32 a = max(max(t[k].x, t[k].y), max(t[k].z, t[k].w)); mx = max(mx, a); }
      if (__all(mx != SENT)) {
#pragma unroll
        for (int k = 0; k < 8; ++k) { const int hd = 8 * w + k; *(LAS v4u*)(L + AL_Q + hd * 1024 + ((lane ^ (hd & 15)) << 4)) = t[k]; }
        break;
      }
      __builtin_amdgcn_s_sleep(2);
    }
    VM0();
    STAMP(2);
  }
  BAR();
  if (w < 4) {
#pragma unroll
    for (int k = 0; k < 16; ++k) S[k] = 0.f;
    {
      v4u a[32], q[32];
#pragma unroll
      for (int s = 0; s < 6; ++s) { a[s] = ldsr128(L + ca[s & 7] + (s >> 3) * 256); q[s] = ldsr128(L + qa[s & 7] + (s >> 3) * 256); }
      __builtin_amdgcn_sched_barrier(0);
#pragma unroll
      for (int s = 0; s < 32; ++s) {
        if (s + 6 < 32) { const int n = s + 6; a[n] = ldsr128(L + ca[n & 7] + (n >> 3) * 256); q[n] = ldsr128(L + qa[n & 7] + (n >> 3) * 256); }
        S = mfma32(a[s], q[s], S);
        __builtin_amdgcn_sched_barrier(0);
      }
    }
    STAMP(3);
    float m0 = fmaxf(fmaxf(S[0], S[1]), fmaxf(S[2], S[3])), m1 = fmaxf(fmaxf(S[4], S[5]), fmaxf(S[6], S[7]));
    float m2 = fmaxf(fmaxf(S[8], S[9]), fmaxf(S[10], S[11])), m3 = fmaxf(fmaxf(S[12], S[13]), fmaxf(S[14], S[15]));
    const float m = x32_max(fmaxf(fmaxf(m0, m1), fmaxf(m2, m3)));
    if (lane < 32) *(LAS float*)(L + AL_MAX + (w * 32 + lane) * 4) = m;
  }
  u32 pa[8], va[4], oa[8];
  {
    const int blk = (lane >> 4) & 1, q = (lane & 15) >> 2, pp = lane & 3;
    const u32 sw = swz(l32);
    const u32 c0 = 8 * (w & 1) + 2 * blk + (pp >> 1);
    const u32 r0 = 8 * hh + q, r1 = r0 + 4, s0 = swz(r0), s1 = swz(r1);
    const u32 tb = AL_CKV + (w >> 1) * 256 + 8 * (pp & 1);
    va[0] = tb + 1024 * r0 + 16 * (c0 ^ s0); va[1] = tb + 1024 * r1 + 16 * (c0 ^ s1);
    va[2] = tb + 1024 * r0 + 16 * ((c0 + 4) ^ s0); va[3] = tb + 1024 * r1 + 16 * ((c0 + 4) ^ s1);
#pragma unroll
    for (int k = 0; k < 8; ++k) { pa[k] = AL_P + l32 * 256 + (((2 * k + hh) ^ sw) << 4); oa[k] = AL_O + l32 * 1024 + (((8 * w + k) ^ (l32 & 15)) << 4) + 8 * hh; PIN(pa[k]); PIN(oa[k]); }
#pragma unroll
    for (int k = 0; k < 4; ++k) PIN(va[k]);
  }
  BAR();
  float M;
  {
    const LAS float* mx = (const LAS float*)(L + AL_MAX) + l32;
    M = fmaxf(fmaxf(mx[0], mx[32]), fmaxf(mx[64], mx[96]));
    M = __uint_as_float(__float_as_uint(M) & 0xFFFFFF00u);
  }
  if (w < 4) {
    float sum = 0.f;
#pragma unroll
    for (int k = 0; k < 16; ++k) { S[k] = __builtin_amdgcn_exp2f(S[k] - M); sum += S[k]; }
    const u32 sw = swz(l32);
#pragma unroll
    for (int qd = 0; qd < 4; ++qd) {
      const v2u o = {pk_bf16(S[4 * qd], S[4 * qd + 1]), pk_bf16(S[4 * qd + 2], S[4 * qd + 3])};
      *(LAS v2u*)(L + AL_P + l32 * 256 + (((4 * w + qd) ^ sw) << 4) + 8 * hh) = o;
    }
    sum = x32_sum(sum);
    if (lane < 32) *(LAS float*)(L + AL_SUM + (w * 32 + lane) * 4) = sum;
  }
  BAR();
  STAMP(4);
  f32x16 O0, O1;
#pragma unroll
  for (int k = 0; k < 16; ++k) { O0[k] = 0.f; O1[k] = 0.f; }
  {
    v4u pb[8], a0[8], a1[8];
#pragma unroll
    for (int ks = 0; ks < 8; ++ks) {
      pb[ks] = ldsr128(L + pa[ks]);
      a0[ks] = tr2(L + va[0] + ks * 16384, L + va[1] + ks * 16384);
      a1[ks] = tr2(L + va[2] + ks * 16384, L + va[3] + ks * 16384);
    }
    if (w == 7) {
      const LAS float* sm = (const LAS float*)(L + AL_SUM) + l32;
      const float Ls = (sm[0] + sm[32]) + (sm[64] + sm[96]);
      const float M2 = dpp<0x128>(M), L2 = dpp<0x128>(Ls);
      wst128(ws, (lane < 32 && !(lane & 8)) ? WS_ST + ((b * 64 + kc) * 8 + (lane & 7)) * 128 + (lane >> 4) * 16 : OOB, tag4(f32x4{M, Ls, M2, L2}, T));
    }
    __builtin_amdgcn_sched_barrier(0);
#pragma unroll
    for (int ks = 0; ks < 8; ++ks) { O0 = mfma32(a0[ks], pb[ks], O0); O1 = mfma32(a1[ks], pb[ks], O1); }
  }
  int s4_o = WS_S4 + ((((4 * w * 4 + g) * 2 + b) * 64 + kc) * 256) + i16 * 16;
  u32 or_a[4];
#pragma unroll
  for (int k = 0; k < 4; ++k) { const int hd = 4 * w + k; or_a[k] = AL_O + hd * 1024 + ((lane ^ (hd & 15)) << 4); PIN(or_a[k]); }
  PIN(s4_o);
  BAR();
  STAMP(5);
#pragma unroll
  for (int qd = 0; qd < 4; ++qd) {
    const v2u o0 = {canon(pk_bf16(O0[4 * qd], O0[4 * qd + 1])), canon(pk_bf16(O0[4 * qd + 2], O0[4 * qd + 3]))};
    const v2u o1 = {canon(pk_bf16(O1[4 * qd], O1[4 * qd + 1])), canon(pk_bf16(O1[4 * qd + 2], O1[4 * qd + 3]))};
    *(LAS v2u*)(L + oa[qd]) = o0;
    *(LAS v2u*)(L + oa[4 + qd]) = o1;
  }
  BAR();
  {
    v4u v[4];
#pragma unroll
    for (int k = 0; k < 4; ++k) v[k] = ldsr128(L + or_a[k]);
#pragma unroll
    for (int k = 0; k < 4; ++k) wst128(ws, s4_o + k * (4 * 2 * 64 * 256), v[k]);
  }
  STAMP(6);
  if (!do_ffn) return;
  const int bb = w >> 2, m0 = 160 * (w & 3) + 4 * (lane < 40 ? lane : 0);
  VM0();
  BAR();
  {
#pragma unroll 1
    for (int n = w; n < 40; n += 8) {
      const int line = n * 8 + (lane >> 3), mat = line >= 160, row = 160 * fsub + line - 160 * mat;
      const char* pp = (const char*)(mat ? P.wu : P.wg) + (size_t)row * 3456 + fl * 128 + (lane & 7) * 16;
      asm volatile("global_load_dwordx4 %0, %1, off" : "+v"(dummy) : "v"(pp) : "memory");
    }
    const char* sd = (const char*)P.wd + (size_t)(16 * fj) * 1280;
    for (int n = w; n < 20; n += 8) dma_s(AL_DN + n * 1024, sd + n * 1024, lane * 16);
    vmwait<0>();
    asm volatile("" :: "v"(dummy));
    const char* gp = (const char*)(((lane & 2) ? P.wu : P.wg)) + (size_t)(lane >> 2) * 3456 + fj * 32 + (lane & 1) * 16;
    for (int n = w; n < 40; n += 8) dma_v(AL_GU + n * 1024, gp + (size_t)n * 16 * 3456);
    vmwait<0>();
  }
  BAR();
  v4u ag[5], ad[5];
  {
#pragma unroll
    for (int i = 0; i < 5; ++i) ag[i] = frag64(L + AL_GU, lane, w & 1, (w >> 1) + 4 * i);
    const int q = (lane & 15) >> 2, pp = lane & 3;
    const int r0 = (8 * g + q) & 15;
#pragma unroll
    for (int k = 0; k < 5; ++k) { const LAS char* a = L + AL_DN + r0 * 1280 + (5 * w + k) * 32 + 8 * pp; ad[k] = tr2(a, a + 4 * 1280); }
  }
  STAMP(7);
  {
    const v2u xv = *(const GAS v2u*)(P.x + bb * 640 + m0);
    v4u mb[3];
    _Pragma("nounroll") for (u32 sp = 0; sp < SPIN_CAP; ++sp) {
      CBAR();
#pragma unroll
      for (int k = 0; k < 3; ++k) mb[k] = wld128(ws, lane < 40 ? WS_MB + ((k * 2 + bb) * 640 + m0) * 4 : OOB);
      if (__all(lane >= 40 || (tbad4(mb[0], T) | tbad4(mb[1], T) | tbad4(mb[2], T)) == 0)) break;
      __builtin_amdgcn_s_sleep(8);
    }
    const f32x4 xf = {bflo(xv.x), bfhi(xv.x), bflo(xv.y), bfhi(xv.y)};
    const f32x4 gav = asf4(mb[0]), sbv = asf4(mb[1]), cb1 = asf4(mb[2]) + 1.f;
    u32 h2_w = AL_H2 + (bb * 640 + m0) * 2, h2_r = AL_H2 + (lane & 1) * 1280 + (32 * (w >> 1) + 8 * g) * 2, red_w = AL_RED + w * 128 + (i16 & 1) * 64 + g * 16,
        red_r = AL_RED + (((lane >> 4) & 1) * 16 + (lane & 15)) * 4, act_w = AL_ACT + w * 128 + (((lane >> 4) & 1) * 32 + (lane >> 5) * 16 + (lane & 15)) * 2,
        act_r = AL_ACT + w * 128 + (lane & 1) * 64 + g * 16, s8_o = i16 < 2 ? WS_S8 + 5 * w * (108 * 128) + fj * 128 + i16 * 64 + g * 16 : OOB;
    int s6_o = lane < 40 ? WS_S6 + cp * S6_COPY + (bb * 640 + m0) * 4 : OOB;
    PIN(h2_w); PIN(h2_r); PIN(red_w); PIN(red_r); PIN(act_w); PIN(act_r); PIN(s8_o); PIN(s6_o);
    f32x4 at;
    _Pragma("nounroll") for (u32 sp = 0; sp < SPIN_CAP; ++sp) {
      CBAR();
      v4u t[8];
#pragma unroll
      for (int k = 0; k < 8; ++k) t[k] = wld128(ws, s6_o + k * 5120);
      at = ((asf4(t[0]) + asf4(t[1])) + (asf4(t[2]) + asf4(t[3]))) + ((asf4(t[4]) + asf4(t[5])) + (asf4(t[6]) + asf4(t[7])));
      u32 bad = 0;
#pragma unroll
      for (int k = 0; k < 8; ++k) bad |= tbad4(t[k], T);
      if (__all(lane >= 40 || bad == 0)) break;
      __builtin_amdgcn_s_sleep(S6_SLEEP);
    }
    const f32x4 x1 = xf + gav * at;
    STAMP(8);
    const bool act = lane < 40;
    const float pv = __builtin_bit_cast(float, __builtin_amdgcn_readfirstlane(__builtin_bit_cast(int, x1[0])));
    const float d0 = x1[0] - pv, d1 = x1[1] - pv, d2 = x1[2] - pv, d3 = x1[3] - pv;
    const float s1 = wave_sum(act ? (d0 + d1) + (d2 + d3) : 0.f);
    const float s2 = wave_sum(act ? (d0 * d0 + d1 * d1) + (d2 * d2 + d3 * d3) : 0.f);
    const float mw = pv + s1 * (1.f / 160.f), m2 = s2 - s1 * s1 * (1.f / 160.f);
    if (lane == 0) { LAS f32x2* sp = (LAS f32x2*)(L + AL_STAT); sp[w] = f32x2{mw, m2}; }
    BAR();
    const LAS f32x4* sp = (const LAS f32x4*)(L + AL_STAT + bb * 32);
    const f32x4 s01 = sp[0], s23 = sp[1];
    const float mean = ((s01[0] + s01[2]) + (s23[0] + s23[2])) * 0.25f;
    const float e0 = s01[0] - mean, e1 = s01[2] - mean, e2 = s23[0] - mean, e3 = s23[2] - mean;
    const float var = (((s01[1] + s01[3]) + (s23[1] + s23[3])) + 160.f * ((e0 * e0 + e1 * e1) + (e2 * e2 + e3 * e3))) * (1.f / 640.f);
    const float rstd = rsqrtf(var + 1e-5f);
    float hv[4];
#pragma unroll
    for (int k = 0; k < 4; ++k) hv[k] = (x1[k] - mean) * rstd * cb1[k] + sbv[k];
    if (act) { const v2u o = {pk_bf16(hv[0], hv[1]), pk_bf16(hv[2], hv[3])}; *(LAS v2u*)(L + h2_w) = o; }
    BAR();
    v4u bh[5];
#pragma unroll
    for (int i = 0; i < 5; ++i) bh[i] = ldsr128(L + h2_r + 256 * i);
    f32x4 a = Z4;
#pragma unroll
    for (int i = 0; i < 5; ++i) a = mfma16(ag[i], bh[i], a);
    if (i16 < 2) *(LAS f32x4*)(L + red_w) = a;
    BAR();
    const LAS float* wr = (const LAS float*)(L + red_r);
    const float gs = (wr[0] + wr[64]) + (wr[128] + wr[192]), us = (wr[32] + wr[96]) + (wr[160] + wr[224]);
    const float av = silu_f(gs) * us;
    *(LAS u16*)(L + act_w) = lane < 32 ? (u16)(pk_bf16(av, 0.f) & 0xFFFFu) : (u16)0;
    LGKM0();
    STAMP(9);
    const v4u bq = ldsr128(L + act_r);
    f32x4 acc[5];
#pragma unroll
    for (int k = 0; k < 5; ++k) acc[k] = mfma16(ad[k], bq, Z4);
#pragma unroll
    for (int k = 0; k < 5; ++k) wst128(ws, s8_o + k * (108 * 128), tag4(acc[k], T));
  }
  STAMP(10);
}

__global__ void __launch_bounds__(512, 2)
fused_decode(const u16* x, const u16* ckv, const u16* wq, const u16* wuk, const u16* wuv, const u16* wo, const u16* wg,
             const u16* wu, const u16* wd, const u16* c, const u16* wmod, u16* out, char* wsp) {
  extern __shared__ __attribute__((aligned(1024))) char lds[];
  LAS char* L = (LAS char*)lds;
  const int lane = threadIdx.x & 63, w = __builtin_amdgcn_readfirstlane(threadIdx.x >> 6);
  const rsrc_t ws = __builtin_amdgcn_make_buffer_rsrc((void*)wsp, 0, WS_END, 0x00020000);
  u64 pc;
  asm volatile("s_getpc_b64 %0" : "=s"(pc));
  Ptrs P{x, ckv, wq, wuk, wuv, wo, wg, wu, wd, c, wmod, out, wsp, (const char*)(pc & ~255ull), __builtin_amdgcn_s_memrealtime()};
  if (threadIdx.x == 0) { *(LAS u32*)(L + L_CNT) = 0u; *(LAS u32*)(L + AL_CNT) = 0u; }
  BAR();
  if (blockIdx.x < 128) f_role(P, L, ws, blockIdx.x, w, lane);
  else a_role(P, L, ws, blockIdx.x - 128, w, lane);

}

extern "C" void kernel_launch(void* const* d_in, const int* in_sizes, int n_in, void* d_out, int out_size, void* d_ws,
                              size_t ws_size, hipStream_t stream) {
  (void)in_sizes; (void)n_in; (void)out_size; (void)ws_size;
  (void)hipFuncSetAttribute((const void*)fused_decode, hipFuncAttributeMaxDynamicSharedMemorySize, LDS_BYTES);
  fused_decode<<<256, 512, LDS_BYTES, stream>>>((const u16*)d_in[0], (const u16*)d_in[1], (const u16*)d_in[2], (const u16*)d_in[3],
                                                (const u16*)d_in[4], (const u16*)d_in[5], (const u16*)d_in[7], (const u16*)d_in[8],
                                                (const u16*)d_in[9], (const u16*)d_in[10], (const u16*)d_in[11], (u16*)d_out, (char*)d_ws);
}
```

```cpp
#include <hip/hip_runtime.h>
#include <hip/hip_bf16.h>
#include <stdint.h>

typedef unsigned short u16;
typedef unsigned int u32;
typedef unsigned long long u64;
typedef u32 v4u __attribute__((ext_vector_type(4)));
typedef u32 v2u __attribute__((ext_vector_type(2)));
typedef float f32x4 __attribute__((ext_vector_type(4)));
typedef float f32x2 __attribute__((ext_vector_type(2)));
typedef float f32x16 __attribute__((ext_vector_type(16)));
typedef short s16x4 __attribute__((ext_vector_type(4)));
typedef short s16x8 __attribute__((ext_vector_type(8)));
typedef __bf16 bf16x8 __attribute__((ext_vector_type(8)));
typedef __bf16 bf16x2 __attribute__((ext_vector_type(2)));
#define GAS __attribute__((address_space(1)))
#define LAS __attribute__((address_space(3)))
#define DI __device__ __forceinline__

#define A_DELAY 0
#define F_TAIL_DELAY 700

#define S6_SLEEP 1
#define S4_SLEEP 8
#define CODE_PF 24576
constexpr u32 SENT = 0xFFFFFFFFu;
constexpr u32 SPIN_CAP = 1u << 17;

constexpr int WS_EPOCH = 0;
constexpr int WS_FLAGF = 256;
constexpr int S1_COPY = 5 * 4 * 640 * 4, S3_COPY = 2 * 32 * 512 * 2, S6_COPY = 8 * 2 * 640 * 4;
constexpr int WS_S1 = WS_FLAGF + 128 * 128 + 3840;
constexpr int WS_S2 = WS_S1 + 8 * S1_COPY;
constexpr int WS_S3 = WS_S2 + 32 * 4 * 1024;
constexpr int WS_S5 = WS_S3 + 8 * S3_COPY;
constexpr int WS_S6 = WS_S5 + 32 * 4 * 1024;
constexpr int WS_MB = WS_S6 + 8 * S6_COPY;
constexpr int WS_S8 = WS_MB + 4 * 2 * 640 * 4;
constexpr int WS_ST = WS_S8 + 40 * 108 * 32 * 4;
constexpr int WS_S4 = WS_ST + 128 * 8 * 128;
constexpr int WS_END = WS_S4 + 32 * 4 * 2 * 64 * 256;
constexpr int WS_STAMP = WS_END;
static_assert(WS_S1 % 256 == 0 && WS_S2 % 256 == 0 && WS_S3 % 256 == 0 && WS_S5 % 256 == 0 && WS_S6 % 256 == 0 && WS_MB % 256 == 0 &&
              WS_S8 % 256 == 0 && WS_ST % 256 == 0 && WS_S4 % 256 == 0, "align");

constexpr int LDS_BYTES = 163840;

typedef __amdgpu_buffer_rsrc_t rsrc_t;
DI v4u wld128(rsrc_t r, int off) { return __builtin_amdgcn_raw_buffer_load_b128(r, off, 0, 16); }
DI v2u wld64(rsrc_t r, int off) { return __builtin_amdgcn_raw_buffer_load_b64(r, off, 0, 16); }
DI u32 wld32(rsrc_t r, int off) { return __builtin_amdgcn_raw_buffer_load_b32(r, off, 0, 16); }
DI void wst128(rsrc_t r, int off, v4u v) { __builtin_amdgcn_raw_buffer_store_b128(v, r, off, 0, 16); }
DI void wst64(rsrc_t r, int off, v2u v) { __builtin_amdgcn_raw_buffer_store_b64(v, r, off, 0, 16); }
DI void wst32(rsrc_t r, int off, u32 v) { __builtin_amdgcn_raw_buffer_store_b32(v, r, off, 0, 16); }
DI u32 canon(u32 x) { return x < 0xFFFFFFFEu ? x : 0xFFFFFFFEu; }
DI bool ok4(v4u v) { return v.x != SENT && v.y != SENT && v.z != SENT && v.w != SENT; }
DI v4u tag4(f32x4 v, u32 T) {
  v4u o;
  o.x = __builtin_amdgcn_perm(__float_as_uint(v[0]), T, 0x07060500u); o.y = __builtin_amdgcn_perm(__float_as_uint(v[1]), T, 0x07060501u);
  o.z = __builtin_amdgcn_perm(__float_as_uint(v[2]), T, 0x07060502u); o.w = __builtin_amdgcn_perm(__float_as_uint(v[3]), T, 0x07060503u);
  return o;
}
DI bool tok4(v4u v, u32 T) { return (__builtin_amdgcn_perm(v.y, v.x, 0x0c0c0400u) | __builtin_amdgcn_perm(v.w, v.z, 0x04000c0cu)) == T; }
DI u32 tbad4(v4u v, u32 T) { return (__builtin_amdgcn_perm(v.y, v.x, 0x0c0c0400u) | __builtin_amdgcn_perm(v.w, v.z, 0x04000c0cu)) ^ T; }
constexpr int OOB = 0x40000000;
DI f32x4 asf4(v4u v) { const v4u m = v & 0xFFFFFF00u; return __builtin_bit_cast(f32x4, m); }
DI u32 mk_tag(u32 epoch_word) { return (epoch_word + 1u) * 0x01010101u; }
#define VM0() do { __builtin_amdgcn_s_waitcnt(0x0F70); asm volatile("" ::: "memory"); } while (0)
DI float bflo(u32 x) { return __uint_as_float(x << 16); }
DI float bfhi(u32 x) { return __uint_as_float(x & 0xffff0000u); }
DI u32 pk_bf16(float lo, float hi) { f32x2 f = {lo, hi}; bf16x2 h = __builtin_convertvector(f, bf16x2); return __builtin_bit_cast(u32, h); }
DI float silu_f(float x) { return x * __builtin_amdgcn_rcpf(1.f + __expf(-x)); }
DI v4u ldnt128(const void* p) { return __builtin_nontemporal_load((const GAS v4u*)p); }
DI f32x4 mfma16(v4u a, v4u b, f32x4 c) { return __builtin_amdgcn_mfma_f32_16x16x32_bf16(__builtin_bit_cast(bf16x8, a), __builtin_bit_cast(bf16x8, b), c, 0, 0, 0); }
DI f32x16 mfma32(v4u a, v4u b, f32x16 c) { return __builtin_amdgcn_mfma_f32_32x32x16_bf16(__builtin_bit_cast(bf16x8, a), __builtin_bit_cast(bf16x8, b), c, 0, 0, 0); }
#define BAR() asm volatile("s_waitcnt lgkmcnt(0)\n\ts_barrier" ::: "memory")
#define CBAR() asm volatile("" ::: "memory")
template <int N> DI void vmwait() { asm volatile("s_waitcnt vmcnt(%0)" :: "i"(N) : "memory"); }

template <int CTRL> DI float dpp(float x) { return __builtin_bit_cast(float, __builtin_amdgcn_mov_dpp(__builtin_bit_cast(int, x), CTRL, 0xf, 0xf, true)); }
DI float x16_sum(float x) { auto s = __builtin_amdgcn_permlane16_swap(__float_as_uint(x), __float_as_uint(x), false, false); return __uint_as_float(s[0]) + __uint_as_float(s[1]); }
DI float x32_sum(float x) { auto s = __builtin_amdgcn_permlane32_swap(__float_as_uint(x), __float_as_uint(x), false, false); return __uint_as_float(s[0]) + __uint_as_float(s[1]); }
DI float x16_max(float x) { auto s = __builtin_amdgcn_permlane16_swap(__float_as_uint(x), __float_as_uint(x), false, false); return fmaxf(__uint_as_float(s[0]), __uint_as_float(s[1])); }
DI float x32_max(float x) { auto s = __builtin_amdgcn_permlane32_swap(__float_as_uint(x), __float_as_uint(x), false, false); return fmaxf(__uint_as_float(s[0]), __uint_as_float(s[1])); }
DI float wave_sum(float x) { x += dpp<0xB1>(x); x += dpp<0x4E>(x); x += dpp<0x141>(x); x += dpp<0x140>(x); return x32_sum(x16_sum(x)); }

DI void dma_s(u32 ldsaddr, const void* sbase, u32 voff) {
  u32 keep;
  asm volatile("s_mov_b32 %0, m0\n\ts_mov_b32 m0, %1\n\ts_nop 0\n\tglobal_load_lds_dwordx4 %2, %3 nt\n\ts_mov_b32 m0, %0"
               : "=&s"(keep) : "s"(ldsaddr), "v"(voff), "s"(sbase) : "memory");
}
DI void dma_s_keep(u32 ldsaddr, const void* sbase, u32 voff) {
  u32 keep;
  asm volatile("s_mov_b32 %0, m0\n\ts_mov_b32 m0, %1\n\ts_nop 0\n\tglobal_load_lds_dwordx4 %2, %3\n\ts_mov_b32 m0, %0"
               : "=&s"(keep) : "s"(ldsaddr), "v"(voff), "s"(sbase) : "memory");
}
DI void dma_v(u32 ldsaddr, const void* vaddr) {
  u32 keep;
  asm volatile("s_mov_b32 %0, m0\n\ts_mov_b32 m0, %1\n\ts_nop 0\n\tglobal_load_lds_dwordx4 %2, off nt\n\ts_mov_b32 m0, %0"
               : "=&s"(keep) : "s"(ldsaddr), "v"(vaddr) : "memory");
}
DI u32 swz(u32 row) { return ((row & 3u) << 2) | ((row >> 2) & 3u); }
DI v4u ldsr128(const LAS char* p) { return *(const LAS v4u*)p; }
DI v4u tr2(const LAS char* p0, const LAS char* p1) {
  s16x4 a = __builtin_amdgcn_ds_read_tr16_b64_v4i16((LAS s16x4*)p0);
  s16x4 b = __builtin_amdgcn_ds_read_tr16_b64_v4i16((LAS s16x4*)p1);
  s16x8 v = __builtin_shufflevector(a, b, 0, 1, 2, 3, 4, 5, 6, 7);
  return __builtin_bit_cast(v4u, v);
}
DI void wait_ticks(u64 t0, u32 ticks) { if (ticks) { while (__builtin_amdgcn_s_memrealtime() - t0 < ticks) __builtin_amdgcn_s_sleep(2); } }

#define STAMP(i) do { } while (0)
#define STARG

struct Ptrs { const u16 *x, *ckv, *wq, *wuk, *wuv, *wo, *wg, *wu, *wd, *c, *wmod; u16* out; char* ws; const char* code; u64 t0; };

DI v4u frag64(const LAS char* img, int lane, int c, int ks) {
  const int g = lane >> 4, q = (lane & 15) >> 2, p = lane & 3;
  const LAS char* a = img + (32 * ks + 8 * g + q) * 64 + 32 * c + 8 * p;
  return tr2(a, a + 4 * 64);
}
DI v4u frag256(const LAS char* img, int lane, int c, int ks) {
  const int g = lane >> 4, q = (lane & 15) >> 2, p = lane & 3;
  const u32 r0 = 8 * g + q, r1 = r0 + 4, ch = 2 * c + (p >> 1);
  return tr2(img + 8192 * ks + 256 * r0 + 16 * (ch ^ swz(r0)) + 8 * (p & 1), img + 8192 * ks + 256 * r1 + 16 * (ch ^ swz(r1)) + 8 * (p & 1));
}
template <int N> DI f32x4 gather_sum(rsrc_t ws, int off, int stride, u32 T) {
  f32x4 s;
  _Pragma("nounroll") for (u32 sp = 0; sp < SPIN_CAP; ++sp) {
    CBAR();
    v4u t[N];
#pragma unroll
    for (int k = 0; k < N; ++k) t[k] = wld128(ws, off + k * stride);
    u32 bad = 0;
#pragma unroll
    for (int k = 0; k < N; ++k) bad |= tbad4(t[k], T);
    s = asf4(t[0]);
#pragma unroll
    for (int k = 1; k < N; ++k) s += asf4(t[k]);
    if (__all(bad == 0)) break;
  }
  return s;
}
template <int SLEEP> DI void wait_piece(rsrc_t ws, int off, u32 T, int lane) {
  _Pragma("nounroll") for (u32 sp = 0; sp < SPIN_CAP; ++sp) {
    CBAR();
    const v4u f = wld128(ws, lane == 0 ? off : OOB);
    if (__builtin_amdgcn_readfirstlane(tbad4(f, T)) == 0) break;
    __builtin_amdgcn_s_sleep(SLEEP);
  }
}
DI u32 lds_ld(const LAS char* p) { return *(const volatile LAS u32*)p; }
#define PIN(x) asm volatile("" : "+v"(x))
DI void lds_wait_ge(const LAS char* p, u32 n) { _Pragma("nounroll") for (u32 sp = 0; sp < (1u << 22); ++sp) { if ((u32)__builtin_amdgcn_readfirstlane(lds_ld(p)) >= n) break; } CBAR(); }
DI void lds_arrive(LAS char* p, int lane) { asm volatile("s_waitcnt lgkmcnt(0)" ::: "memory"); if (lane == 0) __hip_atomic_fetch_add((LAS u32*)p, 1u, __ATOMIC_RELAXED, __HIP_MEMORY_SCOPE_WORKGROUP); }
#define LGKM0() asm volatile("s_waitcnt lgkmcnt(0)" ::: "memory")

constexpr int L_CNT = LDS_BYTES - 64;
constexpr int FL_WQ = 0, FL_WUK = 40960, FL_WO = 0, FL_WUV = 81920, FL_SM = FL_WUV + 32768;
constexpr int FL_HB = FL_SM, FL_QB = FL_HB + 1024, FL_QA = FL_QB + 512, FL_CRED = FL_QA + 512, FL_CST = FL_CRED + 4096,
              FL_CB = FL_CST + 64, FL_CTX = FL_CB + 512, FL_WRED = FL_CTX + 4096, FL_FIN = FL_WRED + 4096, FL_PF = FL_FIN + 1024,
              FL_SB = FL_PF + 1024  ;
static_assert(FL_PF % 16 == 0 && FL_SB + 2560 <= L_CNT, "lds");

DI void f_role(const Ptrs& P, LAS char* L, rsrc_t ws, int p, int w, int lane STARG) {
  const int i16 = lane & 15, g = lane >> 4;
  const int h = p & 31, sl = p >> 5, cp = p & 7;
  const f32x4 Z4 = {0.f, 0.f, 0.f, 0.f};
  float ln[4] = {0.f, 0.f, 0.f, 0.f};
  u32 T = 0;
  if (w < 2) {
    T = wld32(ws, WS_EPOCH);
    {
      const v4u S = {SENT, SENT, SENT, SENT};
#pragma unroll
      for (int k = 0; k < 2; ++k) wst128(ws, WS_S3 + (4 * w + 2 * k + (lane >> 5)) * S3_COPY + (((lane >> 4) & 1) * 32 + h) * 1024 + sl * 256 + i16 * 16, S);
    }
    const v4u xa = *(const GAS v4u*)(P.x + w * 640 + 8 * lane);
    const v4u xb = *(const GAS v4u*)(P.x + w * 640 + 512 + 8 * (lane & 15));
    const v2u xs = *(const GAS v2u*)(P.x + w * 640 + 160 * sl + 4 * (lane < 40 ? lane : 0));
    VM0();
    T = __builtin_amdgcn_readfirstlane(mk_tag(T));
    { const v2u f = {T, ~T}; wst64(ws, lane == 0 ? WS_FLAGF + p * 128 + 8 * w : OOB, f); }
    if (w == 0 && lane == 0) *(LAS u32*)(L + L_CNT + 4) = T;
    float v[16];
#pragma unroll
    for (int k = 0; k < 4; ++k) { v[2 * k] = bflo(xa[k]); v[2 * k + 1] = bfhi(xa[k]); v[8 + 2 * k] = bflo(xb[k]); v[9 + 2 * k] = bfhi(xb[k]); }
    const bool ex = lane < 16;
    float s = 0.f;
#pragma unroll
    for (int k = 0; k < 8; ++k) s += v[k] + (ex ? v[8 + k] : 0.f);
    const float mean = wave_sum(s) * (1.f / 640.f);
    float s2 = 0.f;
#pragma unroll
    for (int k = 0; k < 8; ++k) { const float d = v[k] - mean, e = v[8 + k] - mean; s2 += d * d + (ex ? e * e : 0.f); }
    const float rstd = rsqrtf(wave_sum(s2) * (1.f / 640.f) + 1e-5f);
    ln[0] = (bflo(xs.x) - mean) * rstd; ln[1] = (bfhi(xs.x) - mean) * rstd; ln[2] = (bflo(xs.y) - mean) * rstd; ln[3] = (bfhi(xs.y) - mean) * rstd;
  } else if (w < 6) {
    const char* sb = (const char*)P.wq + (size_t)(160 * sl) * 8192 + h * 256;
    const u32 n0 = w - 2;
    const u32 vo = g * 8192 + ((i16 ^ ((g << 2) | n0)) << 4);
#pragma unroll 2
    for (int k = 0; k < 10; ++k) dma_s(FL_WQ + (n0 + 4 * k) * 1024, sb + (size_t)(n0 + 4 * k) * 4 * 8192, vo);
    vmwait<0>();
    lds_arrive(L + L_CNT, lane);
  } else {
    __builtin_amdgcn_s_sleep(2);
    const char* su = (const char*)P.wuk + (size_t)(128 * sl) * 8192 + h * 256;
    const u32 n0 = w - 6;
    const u32 vo0 = g * 8192 + ((i16 ^ ((g << 2) | n0)) << 4), vo1 = g * 8192 + ((i16 ^ ((g << 2) | (n0 + 2))) << 4);
#pragma unroll 1
    for (int k = 0; k < 8; ++k) {
      dma_s(FL_WUK + (n0 + 4 * k) * 1024, su + (size_t)(n0 + 4 * k) * 4 * 8192, vo0);
      dma_s(FL_WUK + (n0 + 4 * k + 2) * 1024, su + (size_t)(n0 + 4 * k + 2) * 4 * 8192, vo1);
    }
    if (w == 7) {
      const int o0 = (p >> 3) * 1024;
      dma_s_keep(FL_PF, P.code + (o0 < CODE_PF - 1024 ? o0 : CODE_PF - 1024), lane * 16);
      dma_s_keep(FL_PF, P.code + (o0 + 16384 < CODE_PF - 1024 ? o0 + 16384 : CODE_PF - 1024), lane * 16);
    }
  }
  STAMP(1);
  u32 hb_a = FL_HB + (lane & 1) * 320 + g * 16, s2_o = i16 < 2 ? WS_S2 + (h * 4 + sl) * 1024 + w * 128 + i16 * 64 + g * 16 : OOB;
  PIN(hb_a); PIN(s2_o);
  lds_wait_ge(L + L_CNT, 4u);
  v4u aq[5];
#pragma unroll
  for (int k = 0; k < 5; ++k) aq[k] = frag256(L + FL_WQ, lane, w, k);
  STAMP(2);
  if (w < 2) {
    f32x4 sa, ca;
    const int s1_o = lane < 40 ? WS_S1 + cp * S1_COPY + (w * 640 + 160 * sl + 4 * lane) * 4 : OOB;
    _Pragma("nounroll") for (u32 sp = 0; sp < SPIN_CAP; ++sp) {
      CBAR();
      v4u t[10];
#pragma unroll
      for (int k = 0; k < 10; ++k) t[k] = wld128(ws, s1_o + ((k >> 1) * 4 + (k & 1) * 2) * 2560);
      u32 bad = 0;
#pragma unroll
      for (int k = 0; k < 10; ++k) bad |= tbad4(t[k], T);
      sa = asf4(t[0]); ca = asf4(t[1]);
#pragma unroll
      for (int k = 1; k < 5; ++k) { sa += asf4(t[2 * k]); ca += asf4(t[2 * k + 1]); }
      if (__all(lane >= 40 || bad == 0)) break;
    }
    const float h0 = ln[0] * (1.f + ca[0]) + sa[0], h1 = ln[1] * (1.f + ca[1]) + sa[1];
    const float h2 = ln[2] * (1.f + ca[2]) + sa[2], h3 = ln[3] * (1.f + ca[3]) + sa[3];
    if (lane < 40) { const v2u o = {pk_bf16(h0, h1), pk_bf16(h2, h3)}; *(LAS v2u*)(L + FL_HB + w * 320 + lane * 8) = o; }
    STAMP(4);
  }
  BAR();
  {
    v4u bh[5];
#pragma unroll
    for (int k = 0; k < 5; ++k) bh[k] = ldsr128(L + hb_a + 64 * k);
    T = __builtin_amdgcn_readfirstlane(lds_ld(L + L_CNT + 4));
    f32x4 acc = Z4;
#pragma unroll
    for (int k = 0; k < 5; ++k) acc = mfma16(aq[k], bh[k], acc);
    wst128(ws, s2_o, tag4(acc, T));
  }
  STAMP(5);
  u32 qb_a = FL_QB + (lane & 1) * 256 + g * 16, uk_a[4], qa_w = FL_QA + i16 * 256 + (16 * w + 4 * g) * 2;
  {
    const u32 row = 16 * w + i16, sw = swz(row);
#pragma unroll
    for (int k = 0; k < 4; ++k) { uk_a[k] = FL_WUK + row * 256 + (((4 * k + g) ^ sw) << 4); PIN(uk_a[k]); }
  }
  PIN(qb_a); PIN(qa_w);
  if (w == 0) {
    const f32x4 q = gather_sum<4>(ws, WS_S2 + h * 4096 + lane * 16, 1024, T);
    const v2u o = {pk_bf16(q[0], q[1]), pk_bf16(q[2], q[3])};
    *(LAS v2u*)(L + FL_QB + ((lane >> 2) & 1) * 256 + (16 * (lane >> 3) + 4 * (lane & 3)) * 2) = o;
    STAMP(6);
  } else if (w >= 6) {
    vmwait<0>();
  }
  BAR();
  {
    v4u bq[4], au[4];
#pragma unroll
    for (int k = 0; k < 4; ++k) { au[k] = ldsr128(L + uk_a[k]); bq[k] = ldsr128(L + qb_a + 64 * k); }
    f32x4 acc = Z4;
#pragma unroll
    for (int k = 0; k < 4; ++k) acc = mfma16(au[k], bq[k], acc);
    constexpr float SC = 0.08838834764831845f * 1.4426950408889634f;
    if (i16 < 2) { const v2u o = {canon(pk_bf16(acc[0] * SC, acc[1] * SC)), canon(pk_bf16(acc[2] * SC, acc[3] * SC))};
      *(LAS v2u*)(L + qa_w) = o; }
  }
  BAR();
  wst128(ws, lane < 32 ? WS_S3 + w * S3_COPY + ((lane >> 4) * 32 + h) * 1024 + sl * 256 + i16 * 16 : OOB, ldsr128(L + FL_QA + (lane & 31) * 16));
  STAMP(7);
  const int kq = p / 10, ms = p % 10;
  if (w >= 4) {
    if (p < 88) wait_ticks(P.t0, F_TAIL_DELAY);
    const u32 n0 = w - 4;
    const char* sb = (const char*)P.wuv + (size_t)(128 * sl) * 8192 + h * 256;
    const u32 vo = g * 8192 + ((i16 ^ ((g << 2) | n0)) << 4);
#pragma unroll 2
    for (int k = 0; k < 8; ++k) dma_s(FL_WUV + (n0 + 4 * k) * 1024, sb + (size_t)(n0 + 4 * k) * 4 * 8192, vo);
    if (p < 80) {
      const char* so = (const char*)P.wo + (size_t)(512 * kq) * 1280 + ms * 128;
      const u32 vo2 = (lane >> 3) * 1280 + (lane & 7) * 16;
#pragma unroll 2
      for (int n = n0; n < 64; n += 4) dma_s(FL_WO + n * 1024, so + (size_t)n * 8 * 1280, vo2);
    } else if (p >= 88) {
      const char* sm = (const char*)P.wmod + (size_t)(1280 + 64 * (p - 88)) * 2;
      const u32 vo2 = (lane >> 3) * 7680 + (lane & 7) * 16;
#pragma unroll 2
      for (int n = n0; n < 80; n += 4) dma_s(FL_WO + n * 1024, sm + (size_t)n * 8 * 7680, vo2);
    }
    vmwait<0>();
  } else if (p >= 88 && w < 3) {
    const int tid = w * 64 + lane;
    const v4u cvv = *(const GAS v4u*)(P.c + (tid < 160 ? tid : 0) * 8);
    v4u sv;
#pragma unroll
    for (int k = 0; k < 4; ++k) sv[k] = pk_bf16(silu_f(bflo(cvv[k])), silu_f(bfhi(cvv[k])));
    if (tid < 160) *(LAS v4u*)(L + FL_SB + tid * 16) = sv;
  }
  BAR();
  STAMP(8);
  if (p >= 88) {
    f32x4 a[4] = {Z4, Z4, Z4, Z4};
    const int q = (lane & 15) >> 2, pp = lane & 3;
    for (int ks = w; ks < 20; ks += 8) {
      const v4u b = ldsr128(L + FL_SB + (lane & 1) * 1280 + (32 * ks + 8 * g) * 2);
      const LAS char* base = L + FL_WO + (32 * ks + 8 * g + q) * 128 + 8 * pp;
#pragma unroll
      for (int c = 0; c < 4; ++c) a[c] = mfma16(tr2(base + 32 * c, base + 32 * c + 4 * 128), b, a[c]);
    }
    if (i16 < 2) {
      LAS f32x4* wr = (LAS f32x4*)(L + FL_WRED + w * 512 + i16 * 256 + g * 16);
#pragma unroll
      for (int c = 0; c < 4; ++c) wr[4 * c] = a[c];
    }
    BAR();
    if (w == 0) {
      const LAS f32x4* wr = (const LAS f32x4*)(L + FL_WRED + (lane & 31) * 16);
      f32x4 v = wr[0];
#pragma unroll
      for (int k = 1; k < 8; ++k) v += wr[32 * k];
      const int col = 64 * (p - 88);
      wst128(ws, lane < 32 ? WS_MB + (((col / 640) * 2 + (lane >> 4)) * 640 + col % 640) * 4 + (lane & 15) * 16 : OOB, tag4(v, T));
    }
  }
  v4u uv[4], ao[8];
#pragma unroll
  for (int k = 0; k < 4; ++k) uv[k] = frag256(L + FL_WUV, lane, w, k);
  if (p < 80) {
    const LAS char* base = L + FL_WO + (64 * w + 8 * g + ((lane & 15) >> 2)) * 128 + 8 * (lane & 3);
#pragma unroll
    for (int k = 0; k < 8; ++k) { const LAS char* a = base + (k >> 2) * 32 * 128 + (k & 3) * 32; ao[k] = tr2(a, a + 4 * 128); }
  }
  {
    const int b = w >> 2, k0 = 16 * (w & 3);
    const int sto = WS_ST + ((b * 64 + k0 + i16) * 8 + (h & 7)) * 128 + (h >> 4) * 16;
    int s4_o = WS_S4 + ((((h * 4 + sl) * 2 + b) * 64 + k0) * 256) + lane * 16;
    u32 cb_a = FL_CB + (lane & 1) * 256 + g * 16, s5_o = i16 < 2 ? WS_S5 + (h * 4 + sl) * 1024 + w * 128 + i16 * 64 + g * 16 : OOB, cr_a = FL_CRED + w * 512 + (lane & 15) * 32;
    PIN(s4_o); PIN(cb_a); PIN(s5_o); PIN(cr_a);
    if (w == 0) wait_piece<8>(ws, sto, T, lane);
    BAR();
    v4u s;
    _Pragma("nounroll") for (u32 sp = 0; sp < SPIN_CAP; ++sp) {
      CBAR();
      s = wld128(ws, lane < 16 ? sto : OOB);
      if (__all(lane >= 16 || tbad4(s, T) == 0)) break;
      __builtin_amdgcn_s_sleep(2);
    }
    float wj[4];
    {
      const bool odd = (h >> 3) & 1;
      const float m = __uint_as_float((odd ? s.z : s.x) & 0xFFFFFF00u), l = __uint_as_float((odd ? s.w : s.y) & 0xFFFFFF00u);
      float Mw = fmaxf(m, dpp<0xB1>(m)); Mw = fmaxf(Mw, dpp<0x4E>(Mw)); Mw = fmaxf(Mw, dpp<0x141>(Mw)); Mw = fmaxf(Mw, dpp<0x140>(Mw));
      const float e = __builtin_amdgcn_exp2f(m - Mw);
      float Lw = e * l; Lw += dpp<0xB1>(Lw); Lw += dpp<0x4E>(Lw); Lw += dpp<0x141>(Lw); Lw += dpp<0x140>(Lw);
      if (lane == 0) *(LAS f32x2*)(L + FL_CST + w * 8) = f32x2{Mw, Lw};
#pragma unroll
      for (int j = 0; j < 4; ++j) wj[j] = __shfl(e, 4 * j + g);
    }
    STAMP(9);
    __builtin_amdgcn_s_sleep(S4_SLEEP);
    v4u t[4];
    _Pragma("nounroll") for (u32 sp = 0; sp < SPIN_CAP; ++sp) {
      CBAR();
#pragma unroll
      for (int j = 0; j < 4; ++j) t[j] = wld128(ws, s4_o + j * 1024);
      u32 mx = 0;
#pragma unroll
      for (int j = 0; j < 4; ++j) mx = max(mx, max(max(t[j].x, t[j].y), max(t[j].z, t[j].w)));
      if (__all(mx != SENT)) break;
    }
    STAMP(3);
    float acc[8];
#pragma unroll
    for (int k = 0; k < 4; ++k) { acc[2 * k] = wj[0] * bflo(t[0][k]); acc[2 * k + 1] = wj[0] * bfhi(t[0][k]); }
#pragma unroll
    for (int j = 1; j < 4; ++j) {
#pragma unroll
      for (int k = 0; k < 4; ++k) { acc[2 * k] += wj[j] * bflo(t[j][k]); acc[2 * k + 1] += wj[j] * bfhi(t[j][k]); }
    }
#pragma unroll
    for (int k = 0; k < 8; ++k) acc[k] = x32_sum(x16_sum(acc[k]));
    if (lane < 16) {
      LAS f32x4* cr = (LAS f32x4*)(L + cr_a);
      cr[0] = f32x4{acc[0], acc[1], acc[2], acc[3]}; cr[1] = f32x4{acc[4], acc[5], acc[6], acc[7]};
    }
    BAR();
    if (w == 0) {
      const int bb = lane >> 5;
      const LAS f32x4* cs = (const LAS f32x4*)(L + FL_CST + bb * 32);
      const f32x4 c01 = cs[0], c23 = cs[1];
      const LAS f32x4* cr = (const LAS f32x4*)(L + FL_CRED + bb * 2048 + (lane & 31) * 16);
      const f32x4 r0 = cr[0], r1 = cr[32], r2 = cr[64], r3 = cr[96];
      const float M = fmaxf(fmaxf(c01[0], c01[2]), fmaxf(c23[0], c23[2]));
      const float f0 = __builtin_amdgcn_exp2f(c01[0] - M), f1 = __builtin_amdgcn_exp2f(c01[2] - M), f2 = __builtin_amdgcn_exp2f(c23[0] - M), f3 = __builtin_amdgcn_exp2f(c23[2] - M);
      const float inv = __builtin_amdgcn_rcpf((f0 * c01[1] + f1 * c01[3]) + (f2 * c23[1] + f3 * c23[3]));
      const f32x4 v = ((r0 * f0 + r1 * f1) + (r2 * f2 + r3 * f3)) * inv;
      const v2u o = {pk_bf16(v[0], v[1]), pk_bf16(v[2], v[3])};
      *(LAS v2u*)(L + FL_CB + lane * 8) = o;
    }
    BAR();
    v4u bc[4];
#pragma unroll
    for (int k = 0; k < 4; ++k) bc[k] = ldsr128(L + cb_a + 64 * k);
    f32x4 a2 = Z4;
#pragma unroll
    for (int k = 0; k < 4; ++k) a2 = mfma16(uv[k], bc[k], a2);
    wst128(ws, s5_o, tag4(a2, T));
  }
  STAMP(10);
  if (p < 80) {
    u32 cx_w = FL_CTX + w * 256 + ((lane >> 2) & 1) * 128 + (16 * ((lane & 31) >> 3) + 4 * (lane & 3)) * 2, cx_r = FL_CTX + w * 256 + (lane & 1) * 128 + g * 16,
        wr_w = FL_WRED + w * 512 + (i16 & 1) * 256 + g * 16, wr_r = FL_WRED + (lane & 31) * 16,
        s6_o = lane < 32 ? WS_S6 + w * S6_COPY + ((kq * 2 + (lane >> 4)) * 640 + 64 * ms) * 4 + (lane & 15) * 16 : OOB;
    PIN(cx_w); PIN(cx_r); PIN(wr_w); PIN(wr_r); PIN(s6_o);
    f32x4 cx = gather_sum<2>(ws, WS_S5 + (4 * kq + (w >> 1)) * 4096 + (lane >> 5) * 1024 + (w & 1) * 512 + (lane & 31) * 16, 2048, T);
    STAMP(11);
#pragma unroll
    for (int k = 0; k < 4; ++k) cx[k] = x32_sum(cx[k]);
    const v2u o = {pk_bf16(cx[0], cx[1]), pk_bf16(cx[2], cx[3])};
    if (lane < 32) *(LAS v2u*)(L + cx_w) = o;
    LGKM0();
    v4u bx[2];
#pragma unroll
    for (int k = 0; k < 2; ++k) bx[k] = ldsr128(L + cx_r + 64 * k);
    f32x4 a[4];
#pragma unroll
    for (int c = 0; c < 4; ++c) a[c] = mfma16(ao[4 + c], bx[1], mfma16(ao[c], bx[0], Z4));
    if (i16 < 2) {
      LAS f32x4* wr = (LAS f32x4*)(L + wr_w);
#pragma unroll
      for (int c = 0; c < 4; ++c) wr[4 * c] = a[c];
    }
    BAR();
    {
      const LAS f32x4* wr = (const LAS f32x4*)(L + wr_r);
      f32x4 v[8];
#pragma unroll
      for (int k = 0; k < 8; ++k) v[k] = wr[32 * k];
      const f32x4 r = ((v[0] + v[1]) + (v[2] + v[3])) + ((v[4] + v[5]) + (v[6] + v[7]));
      wst128(ws, s6_o, tag4(r, T));
    }
    STAMP(12);
  } else if (p >= 88) {
    const int fs = p - 88, bm = lane & 7, bb = bm >> 2, m0 = 16 * fs + 4 * (bm & 3);
    f32x4 x1, gb;
    if (w == 0) {
      const v2u xv = *(const GAS v2u*)(P.x + bb * 640 + m0);
      _Pragma("nounroll") for (u32 sp = 0; sp < SPIN_CAP; ++sp) {
        CBAR();
        const v4u t0 = wld128(ws, WS_S6 + cp * S6_COPY + (((lane >> 3) * 2 + bb) * 640 + m0) * 4);
        const v4u t4 = wld128(ws, lane < 8 ? WS_MB + ((0 * 2 + bb) * 640 + m0) * 4 : OOB);
        const v4u t5 = wld128(ws, lane < 8 ? WS_MB + ((3 * 2 + bb) * 640 + m0) * 4 : OOB);
        const u32 bad = tbad4(t0, T) | (lane < 8 ? tbad4(t4, T) | tbad4(t5, T) : 0u);
        f32x4 at = asf4(t0);
#pragma unroll
        for (int k = 0; k < 4; ++k) { float v = at[k]; v += dpp<0x128>(v); at[k] = x32_sum(x16_sum(v)); }
        const f32x4 xf = {bflo(xv.x), bfhi(xv.x), bflo(xv.y), bfhi(xv.y)};
        x1 = xf + asf4(t4) * at;
        gb = asf4(t5);
        if (__all(bad == 0)) break;
        __builtin_amdgcn_s_sleep(4);
      }
    }
    BAR();
    STAMP(11);
    f32x4 y;
    const int tid = w * 64 + lane;
    _Pragma("nounroll") for (u32 sp = 0; sp < SPIN_CAP; ++sp) {
      CBAR();
      const v4u t0 = wld128(ws, WS_S8 + fs * (108 * 128) + tid * 16);
      const v4u t1 = wld128(ws, WS_S8 + fs * (108 * 128) + (tid < 352 ? 512 + tid : tid) * 16);
      y = asf4(t0);
      if (tid < 352) y += asf4(t1);
      if (__all((tbad4(t0, T) | tbad4(t1, T)) == 0)) break;
      __builtin_amdgcn_s_sleep(1);
    }
    STAMP(12);
#pragma unroll
    for (int k = 0; k < 4; ++k) { float v = y[k]; v += dpp<0x128>(v); y[k] = x32_sum(x16_sum(v)); }
    if (lane < 8) *(LAS f32x4*)(L + FL_FIN + w * 128 + lane * 16) = y;
    BAR();
    if (w == 0) {
      const LAS f32x4* fr = (const LAS f32x4*)(L + FL_FIN + bm * 16);
      f32x4 v[8];
#pragma unroll
      for (int k = 0; k < 8; ++k) v[k] = fr[8 * k];
      const f32x4 o = x1 + gb * (((v[0] + v[1]) + (v[2] + v[3])) + ((v[4] + v[5]) + (v[6] + v[7])));
      const v2u ob = {pk_bf16(o[0], o[1]), pk_bf16(o[2], o[3])};
      v2u ob2 = ob;
      if (p == 88) {
        const rsrc_t ws2 = __builtin_amdgcn_make_buffer_rsrc((void*)P.ws, 0, WS_END + 65536, 0x00020000);
        const u32 now = (u32)__builtin_amdgcn_s_memrealtime();
        u32 mn = 0xFFFFFFFFu, mx = 0;
        for (int k = 0; k < 4; ++k) { const u32 d = now - wld32(ws2, WS_END + (k * 64 + lane) * 128); mn = min(mn, d); mx = max(mx, d); }
        for (int o = 32; o > 0; o >>= 1) { mn = min(mn, (u32)__shfl_xor((int)mn, o)); mx = max(mx, (u32)__shfl_xor((int)mx, o)); }
        const u32 val = 0 ? mx - mn : mx;
        if (lane == 0 && val > 2200) ob2.x = (ob2.x & 0xFFFF0000u) | (pk_bf16((float)val * 8.f, 0.f) & 0xFFFFu);
      }
      if (lane < 8) *(GAS v2u*)(P.out + bb * 640 + m0) = ob2;
      if (p == 88 && lane == 0) wst32(ws, WS_EPOCH, wld32(ws, WS_EPOCH) + 1u);
    }
    STAMP(13);
  }
}

constexpr int AL_CKV = 0, AL_Q = 131072;
constexpr int AL_P = AL_Q, AL_MAX = AL_Q + 8192, AL_SUM = AL_MAX + 512;
constexpr int AL_O = 0, AL_GU = 32768, AL_DN = AL_Q + 10240  , AL_SM = AL_GU + 40960;
constexpr int AL_H2 = AL_SM, AL_STAT = AL_H2 + 2560, AL_RED = AL_STAT + 64, AL_ACT = AL_RED + 1024;
constexpr int AL_CNT = AL_Q + 4096;
static_assert(AL_ACT + 8 * 128 <= AL_Q && AL_SUM + 512 <= AL_DN && AL_DN + 20480 <= LDS_BYTES, "lds");

DI void a_role(const Ptrs& P, LAS char* L, rsrc_t ws, int j, int w, int lane STARG) {
  const int i16 = lane & 15, g = lane >> 4, l32 = lane & 31, hh = lane >> 5;
  const int b = j >> 6, kc = j & 63, cp = j & 7;
  const f32x4 Z4 = {0.f, 0.f, 0.f, 0.f};
  f32x16 S;
  const int fl = (j & 7) + 8 * (j >> 5), fsub = (j >> 3) & 3, fj = 4 * fl + fsub;
  const bool do_ffn = fl < 27;
  v4u dummy = {0u, 0u, 0u, 0u};
  u32 T = 0;
  u32 ca[8], qa[8];
  T = wld32(ws, WS_EPOCH);
  {
    const int un = (j & 7) + 8 * (j >> 4), cg = 2 * (un / 5) + ((j >> 3) & 1), ks1 = un % 5;
    if (un < 50) {
      const int base = 128 * ks1 + 16 * w;
      const int bb = (lane >> 3) & 1, r1 = i16 >> 3, mat = (i16 >> 2) & 1, ch = i16 & 3, jj = lane & 7;
      u32 cv[2]; v4u wm[2];
#pragma unroll
      for (int t = 0; t < 2; ++t) cv[t] = *(const GAS u32*)(P.c + bb * 640 + base + 8 * t + 2 * g);
#pragma unroll
      for (int t = 0; t < 2; ++t) wm[t] = ldnt128(P.wmod + (size_t)(base + 8 * t + 2 * g + r1) * 3840 + mat * 640 + 32 * cg + 8 * ch);
      u32 msk[4];
#pragma unroll
      for (int r = 0; r < 4; ++r) msk[r] = (r == (jj >> 1)) ? (0xFFFFu << (16 * (jj & 1))) : 0u;
      int s1_o = WS_S1 + w * S1_COPY + ((ks1 * 4 + (l32 >> 3)) * 640 + 32 * cg) * 4 + (lane & 7) * 16;
      if (lane >= 32) s1_o = OOB;
      PIN(s1_o);
      f32x4 a0 = Z4, a1 = Z4;
#pragma unroll
      for (int t = 0; t < 2; ++t) {
        const u32 sp = pk_bf16(silu_f(bflo(cv[t])), silu_f(bfhi(cv[t])));
        const u32 d0 = (sp & 0xFFFFu) * 0x10001u, d1 = (sp >> 16) * 0x10001u;
        const v4u B0 = {d0 & msk[0], d0 & msk[1], d0 & msk[2], d0 & msk[3]};
        const v4u B1 = {d1 & msk[0], d1 & msk[1], d1 & msk[2], d1 & msk[3]};
        a0 = mfma16(wm[t], B0, a0);
        a1 = mfma16(wm[t], B1, a1);
      }
      LAS float* red = (LAS float*)(L + AL_Q) + w * 128;
      const int mt = (lane >> 4) & 1;
#pragma unroll
      for (int r = 0; r < 4; ++r) {
        const float v = x32_sum(lane < 32 ? a0[r] : a1[r]);
        if (lane < 32) red[(mt * 2 + bb) * 32 + 8 * r + jj] = v;
      }
      T = __builtin_amdgcn_readfirstlane(mk_tag(T));
      BAR();
      {
        const LAS f32x4* rd = (const LAS f32x4*)(L + AL_Q) + l32;
        const f32x4 s = ((rd[0] + rd[32]) + (rd[64] + rd[96])) + ((rd[128] + rd[160]) + (rd[192] + rd[224]));
        wst128(ws, s1_o, tag4(s, T));
      }
      BAR();
    } else {
      T = __builtin_amdgcn_readfirstlane(mk_tag(T));
    }
  }
  STAMP(14);
  if (w >= 4) {
    const int lw = w - 4;
    const char* sb = (const char*)P.ckv + ((size_t)(b * 8192 + kc * 128 + 32 * lw)) * 1024;
    wait_ticks(P.t0, A_DELAY);
#pragma unroll 1
    for (int i4 = 0; i4 < 2; ++i4) {
#pragma unroll
      for (int i = 0; i < 16; ++i) {
        const u32 sw = ((i & 3) << 2) | ((i >> 2) & 3);
        dma_s(AL_CKV + (32 * lw + 16 * i4 + i) * 1024, sb + (16 * i4 + i) * 1024, g * 256 + ((i16 ^ sw) << 4));
      }
    }
    STAMP(1);
    vmwait<0>();
  } else {
    {
      const v4u Sv = {SENT, SENT, SENT, SENT};
#pragma unroll
      for (int k = 0; k < 8; ++k) wst128(ws, WS_S4 + (((((8 * w + k) * 4 + g) * 2 + b) * 64 + kc) * 256) + i16 * 16, Sv);
    }
    {
      const u32 row = 32 * w + l32, sw = swz(row);
#pragma unroll
      for (int s = 0; s < 8; ++s) { ca[s] = AL_CKV + row * 1024 + (((2 * s + hh) ^ sw) << 4); qa[s] = AL_Q + l32 * 1024 + (((2 * s + hh) ^ (l32 & 15)) << 4); PIN(ca[s]); PIN(qa[s]); }
    }
    _Pragma("nounroll") for (u32 sp = 0; sp < SPIN_CAP; ++sp) {
      CBAR();
      const v4u f = wld128(ws, lane < 32 ? WS_FLAGF + (8 * w + (lane & 7) + 32 * (lane >> 3)) * 128 : OOB);
      if (__all(lane >= 32 || ((f.x ^ T) | (f.y ^ ~T) | (f.z ^ T) | (f.w ^ ~T)) == 0)) break;
      __builtin_amdgcn_s_sleep(8);
    }
    STAMP(1);
    const int s3_o = WS_S3 + cp * S3_COPY + (b * 32 + 8 * w) * 1024;
    _Pragma("nounroll") for (u32 sp = 0; sp < SPIN_CAP; ++sp) {
      CBAR();
      const u32 f = wld32(ws, lane == 0 ? s3_o : OOB);
      if (__builtin_amdgcn_readfirstlane(f) != SENT) break;
      __builtin_amdgcn_s_sleep(4);
    }
    _Pragma("nounroll") for (u32 sp = 0; sp < SPIN_CAP; ++sp) {
      CBAR();
      v4u t[8];
#pragma unroll
      for (int k = 0; k < 8; ++k) t[k] = wld128(ws, s3_o + k * 1024 + lane * 16);
      u32 mx = 0;
#pragma unroll
      for (int k = 0; k < 8; ++k) { const u32 a = max(max(t[k].x, t[k].y), max(t[k].z, t[k].w)); mx = max(mx, a); }
      if (__all(mx != SENT)) {
#pragma unroll
        for (int k = 0; k < 8; ++k) { const int hd = 8 * w + k; *(LAS v4u*)(L + AL_Q + hd * 1024 + ((lane ^ (hd & 15)) << 4)) = t[k]; }
        break;
      }
      __builtin_amdgcn_s_sleep(2);
    }
    VM0();
    STAMP(2);
  }
  BAR();
  if (w < 4) {
#pragma unroll
    for (int k = 0; k < 16; ++k) S[k] = 0.f;
    {
      v4u a[32], q[32];
#pragma unroll
      for (int s = 0; s < 6; ++s) { a[s] = ldsr128(L + ca[s & 7] + (s >> 3) * 256); q[s] = ldsr128(L + qa[s & 7] + (s >> 3) * 256); }
      __builtin_amdgcn_sched_barrier(0);
#pragma unroll
      for (int s = 0; s < 32; ++s) {
        if (s + 6 < 32) { const int n = s + 6; a[n] = ldsr128(L + ca[n & 7] + (n >> 3) * 256); q[n] = ldsr128(L + qa[n & 7] + (n >> 3) * 256); }
        S = mfma32(a[s], q[s], S);
        __builtin_amdgcn_sched_barrier(0);
      }
    }
    STAMP(3);
    float m0 = fmaxf(fmaxf(S[0], S[1]), fmaxf(S[2], S[3])), m1 = fmaxf(fmaxf(S[4], S[5]), fmaxf(S[6], S[7]));
    float m2 = fmaxf(fmaxf(S[8], S[9]), fmaxf(S[10], S[11])), m3 = fmaxf(fmaxf(S[12], S[13]), fmaxf(S[14], S[15]));
    const float m = x32_max(fmaxf(fmaxf(m0, m1), fmaxf(m2, m3)));
    if (lane < 32) *(LAS float*)(L + AL_MAX + (w * 32 + lane) * 4) = m;
  }
  u32 pa[8], va[4], oa[8];
  {
    const int blk = (lane >> 4) & 1, q = (lane & 15) >> 2, pp = lane & 3;
    const u32 sw = swz(l32);
    const u32 c0 = 8 * (w & 1) + 2 * blk + (pp >> 1);
    const u32 r0 = 8 * hh + q, r1 = r0 + 4, s0 = swz(r0), s1 = swz(r1);
    const u32 tb = AL_CKV + (w >> 1) * 256 + 8 * (pp & 1);
    va[0] = tb + 1024 * r0 + 16 * (c0 ^ s0); va[1] = tb + 1024 * r1 + 16 * (c0 ^ s1);
    va[2] = tb + 1024 * r0 + 16 * ((c0 + 4) ^ s0); va[3] = tb + 1024 * r1 + 16 * ((c0 + 4) ^ s1);
#pragma unroll
    for (int k = 0; k < 8; ++k) { pa[k] = AL_P + l32 * 256 + (((2 * k + hh) ^ sw) << 4); oa[k] = AL_O + l32 * 1024 + (((8 * w + k) ^ (l32 & 15)) << 4) + 8 * hh; PIN(pa[k]); PIN(oa[k]); }
#pragma unroll
    for (int k = 0; k < 4; ++k) PIN(va[k]);
  }
  BAR();
  float M;
  {
    const LAS float* mx = (const LAS float*)(L + AL_MAX) + l32;
    M = fmaxf(fmaxf(mx[0], mx[32]), fmaxf(mx[64], mx[96]));
    M = __uint_as_float(__float_as_uint(M) & 0xFFFFFF00u);
  }
  if (w < 4) {
    float sum = 0.f;
#pragma unroll
    for (int k = 0; k < 16; ++k) { S[k] = __builtin_amdgcn_exp2f(S[k] - M); sum += S[k]; }
    const u32 sw = swz(l32);
#pragma unroll
    for (int qd = 0; qd < 4; ++qd) {
      const v2u o = {pk_bf16(S[4 * qd], S[4 * qd + 1]), pk_bf16(S[4 * qd + 2], S[4 * qd + 3])};
      *(LAS v2u*)(L + AL_P + l32 * 256 + (((4 * w + qd) ^ sw) << 4) + 8 * hh) = o;
    }
    sum = x32_sum(sum);
    if (lane < 32) *(LAS float*)(L + AL_SUM + (w * 32 + lane) * 4) = sum;
  }
  BAR();
  STAMP(4);
  f32x16 O0, O1;
#pragma unroll
  for (int k = 0; k < 16; ++k) { O0[k] = 0.f; O1[k] = 0.f; }
  {
    v4u pb[8], a0[8], a1[8];
#pragma unroll
    for (int ks = 0; ks < 8; ++ks) {
      pb[ks] = ldsr128(L + pa[ks]);
      a0[ks] = tr2(L + va[0] + ks * 16384, L + va[1] + ks * 16384);
      a1[ks] = tr2(L + va[2] + ks * 16384, L + va[3] + ks * 16384);
    }
    if (w == 7) {
      const LAS float* sm = (const LAS float*)(L + AL_SUM) + l32;
      const float Ls = (sm[0] + sm[32]) + (sm[64] + sm[96]);
      const float M2 = dpp<0x128>(M), L2 = dpp<0x128>(Ls);
      wst128(ws, (lane < 32 && !(lane & 8)) ? WS_ST + ((b * 64 + kc) * 8 + (lane & 7)) * 128 + (lane >> 4) * 16 : OOB, tag4(f32x4{M, Ls, M2, L2}, T));
    }
    __builtin_amdgcn_sched_barrier(0);
#pragma unroll
    for (int ks = 0; ks < 8; ++ks) { O0 = mfma32(a0[ks], pb[ks], O0); O1 = mfma32(a1[ks], pb[ks], O1); }
  }
  int s4_o = WS_S4 + ((((4 * w * 4 + g) * 2 + b) * 64 + kc) * 256) + i16 * 16;
  u32 or_a[4];
#pragma unroll
  for (int k = 0; k < 4; ++k) { const int hd = 4 * w + k; or_a[k] = AL_O + hd * 1024 + ((lane ^ (hd & 15)) << 4); PIN(or_a[k]); }
  PIN(s4_o);
  BAR();
  STAMP(5);
#pragma unroll
  for (int qd = 0; qd < 4; ++qd) {
    const v2u o0 = {canon(pk_bf16(O0[4 * qd], O0[4 * qd + 1])), canon(pk_bf16(O0[4 * qd + 2], O0[4 * qd + 3]))};
    const v2u o1 = {canon(pk_bf16(O1[4 * qd], O1[4 * qd + 1])), canon(pk_bf16(O1[4 * qd + 2], O1[4 * qd + 3]))};
    *(LAS v2u*)(L + oa[qd]) = o0;
    *(LAS v2u*)(L + oa[4 + qd]) = o1;
  }
  BAR();
  {
    v4u v[4];
#pragma unroll
    for (int k = 0; k < 4; ++k) v[k] = ldsr128(L + or_a[k]);
#pragma unroll
    for (int k = 0; k < 4; ++k) wst128(ws, s4_o + k * (4 * 2 * 64 * 256), v[k]);
  }
  STAMP(6);
  if (!do_ffn) return;
  const int bb = w >> 2, m0 = 160 * (w & 3) + 4 * (lane < 40 ? lane : 0);
  VM0();
  BAR();
  {
#pragma unroll 1
    for (int n = w; n < 40; n += 8) {
      const int line = n * 8 + (lane >> 3), mat = line >= 160, row = 160 * fsub + line - 160 * mat;
      const char* pp = (const char*)(mat ? P.wu : P.wg) + (size_t)row * 3456 + fl * 128 + (lane & 7) * 16;
      asm volatile("global_load_dwordx4 %0, %1, off" : "+v"(dummy) : "v"(pp) : "memory");
    }
    const char* sd = (const char*)P.wd + (size_t)(16 * fj) * 1280;
    for (int n = w; n < 20; n += 8) dma_s(AL_DN + n * 1024, sd + n * 1024, lane * 16);
    vmwait<0>();
    asm volatile("" :: "v"(dummy));
    const char* gp = (const char*)(((lane & 2) ? P.wu : P.wg)) + (size_t)(lane >> 2) * 3456 + fj * 32 + (lane & 1) * 16;
    for (int n = w; n < 40; n += 8) dma_v(AL_GU + n * 1024, gp + (size_t)n * 16 * 3456);
    vmwait<0>();
  }
  BAR();
  v4u ag[5], ad[5];
  {
#pragma unroll
    for (int i = 0; i < 5; ++i) ag[i] = frag64(L + AL_GU, lane, w & 1, (w >> 1) + 4 * i);
    const int q = (lane & 15) >> 2, pp = lane & 3;
    const int r0 = (8 * g + q) & 15;
#pragma unroll
    for (int k = 0; k < 5; ++k) { const LAS char* a = L + AL_DN + r0 * 1280 + (5 * w + k) * 32 + 8 * pp; ad[k] = tr2(a, a + 4 * 1280); }
  }
  STAMP(7);
  {
    const v2u xv = *(const GAS v2u*)(P.x + bb * 640 + m0);
    v4u mb[3];
    _Pragma("nounroll") for (u32 sp = 0; sp < SPIN_CAP; ++sp) {
      CBAR();
#pragma unroll
      for (int k = 0; k < 3; ++k) mb[k] = wld128(ws, lane < 40 ? WS_MB + ((k * 2 + bb) * 640 + m0) * 4 : OOB);
      if (__all(lane >= 40 || (tbad4(mb[0], T) | tbad4(mb[1], T) | tbad4(mb[2], T)) == 0)) break;
      __builtin_amdgcn_s_sleep(8);
    }
    const f32x4 xf = {bflo(xv.x), bfhi(xv.x), bflo(xv.y), bfhi(xv.y)};
    const f32x4 gav = asf4(mb[0]), sbv = asf4(mb[1]), cb1 = asf4(mb[2]) + 1.f;
    u32 h2_w = AL_H2 + (bb * 640 + m0) * 2, h2_r = AL_H2 + (lane & 1) * 1280 + (32 * (w >> 1) + 8 * g) * 2, red_w = AL_RED + w * 128 + (i16 & 1) * 64 + g * 16,
        red_r = AL_RED + (((lane >> 4) & 1) * 16 + (lane & 15)) * 4, act_w = AL_ACT + w * 128 + (((lane >> 4) & 1) * 32 + (lane >> 5) * 16 + (lane & 15)) * 2,
        act_r = AL_ACT + w * 128 + (lane & 1) * 64 + g * 16, s8_o = i16 < 2 ? WS_S8 + 5 * w * (108 * 128) + fj * 128 + i16 * 64 + g * 16 : OOB;
    int s6_o = lane < 40 ? WS_S6 + cp * S6_COPY + (bb * 640 + m0) * 4 : OOB;
    PIN(h2_w); PIN(h2_r); PIN(red_w); PIN(red_r); PIN(act_w); PIN(act_r); PIN(s8_o); PIN(s6_o);
    f32x4 at;
    _Pragma("nounroll") for (u32 sp = 0; sp < SPIN_CAP; ++sp) {
      CBAR();
      v4u t[8];
#pragma unroll
      for (int k = 0; k < 8; ++k) t[k] = wld128(ws, s6_o + k * 5120);
      at = ((asf4(t[0]) + asf4(t[1])) + (asf4(t[2]) + asf4(t[3]))) + ((asf4(t[4]) + asf4(t[5])) + (asf4(t[6]) + asf4(t[7])));
      u32 bad = 0;
#pragma unroll
      for (int k = 0; k < 8; ++k) bad |= tbad4(t[k], T);
      if (__all(lane >= 40 || bad == 0)) break;
      __builtin_amdgcn_s_sleep(S6_SLEEP);
    }
    const f32x4 x1 = xf + gav * at;
    STAMP(8);
    const bool act = lane < 40;
    const float pv = __builtin_bit_cast(float, __builtin_amdgcn_readfirstlane(__builtin_bit_cast(int, x1[0])));
    const float d0 = x1[0] - pv, d1 = x1[1] - pv, d2 = x1[2] - pv, d3 = x1[3] - pv;
    const float s1 = wave_sum(act ? (d0 + d1) + (d2 + d3) : 0.f);
    const float s2 = wave_sum(act ? (d0 * d0 + d1 * d1) + (d2 * d2 + d3 * d3) : 0.f);
    const float mw = pv + s1 * (1.f / 160.f), m2 = s2 - s1 * s1 * (1.f / 160.f);
    if (lane == 0) { LAS f32x2* sp = (LAS f32x2*)(L + AL_STAT); sp[w] = f32x2{mw, m2}; }
    BAR();
    const LAS f32x4* sp = (const LAS f32x4*)(L + AL_STAT + bb * 32);
    const f32x4 s01 = sp[0], s23 = sp[1];
    const float mean = ((s01[0] + s01[2]) + (s23[0] + s23[2])) * 0.25f;
    const float e0 = s01[0] - mean, e1 = s01[2] - mean, e2 = s23[0] - mean, e3 = s23[2] - mean;
    const float var = (((s01[1] + s01[3]) + (s23[1] + s23[3])) + 160.f * ((e0 * e0 + e1 * e1) + (e2 * e2 + e3 * e3))) * (1.f / 640.f);
    const float rstd = rsqrtf(var + 1e-5f);
    float hv[4];
#pragma unroll
    for (int k = 0; k < 4; ++k) hv[k] = (x1[k] - mean) * rstd * cb1[k] + sbv[k];
    if (act) { const v2u o = {pk_bf16(hv[0], hv[1]), pk_bf16(hv[2], hv[3])}; *(LAS v2u*)(L + h2_w) = o; }
    BAR();
    v4u bh[5];
#pragma unroll
    for (int i = 0; i < 5; ++i) bh[i] = ldsr128(L + h2_r + 256 * i);
    f32x4 a = Z4;
#pragma unroll
    for (int i = 0; i < 5; ++i) a = mfma16(ag[i], bh[i], a);
    if (i16 < 2) *(LAS f32x4*)(L + red_w) = a;
    BAR();
    const LAS float* wr = (const LAS float*)(L + red_r);
    const float gs = (wr[0] + wr[64]) + (wr[128] + wr[192]), us = (wr[32] + wr[96]) + (wr[160] + wr[224]);
    const float av = silu_f(gs) * us;
    *(LAS u16*)(L + act_w) = lane < 32 ? (u16)(pk_bf16(av, 0.f) & 0xFFFFu) : (u16)0;
    LGKM0();
    STAMP(9);
    const v4u bq = ldsr128(L + act_r);
    f32x4 acc[5];
#pragma unroll
    for (int k = 0; k < 5; ++k) acc[k] = mfma16(ad[k], bq, Z4);
#pragma unroll
    for (int k = 0; k < 5; ++k) wst128(ws, s8_o + k * (108 * 128), tag4(acc[k], T));
  }
  STAMP(10);
}

__global__ void __launch_bounds__(512, 2)
fused_decode(const u16* x, const u16* ckv, const u16* wq, const u16* wuk, const u16* wuv, const u16* wo, const u16* wg,
             const u16* wu, const u16* wd, const u16* c, const u16* wmod, u16* out, char* wsp) {
  extern __shared__ __attribute__((aligned(1024))) char lds[];
  LAS char* L = (LAS char*)lds;
  const int lane = threadIdx.x & 63, w = __builtin_amdgcn_readfirstlane(threadIdx.x >> 6);
  const rsrc_t ws = __builtin_amdgcn_make_buffer_rsrc((void*)wsp, 0, WS_END, 0x00020000);
  u64 pc;
  asm volatile("s_getpc_b64 %0" : "=s"(pc));
  Ptrs P{x, ckv, wq, wuk, wuv, wo, wg, wu, wd, c, wmod, out, wsp, (const char*)(pc & ~255ull), __builtin_amdgcn_s_memrealtime()};
  { const rsrc_t ws2 = __builtin_amdgcn_make_buffer_rsrc((void*)wsp, 0, WS_END + 65536, 0x00020000); if (threadIdx.x == 192) wst32(ws2, WS_END + blockIdx.x * 128, (u32)P.t0); }
  if (threadIdx.x == 0) { *(LAS u32*)(L + L_CNT) = 0u; *(LAS u32*)(L + AL_CNT) = 0u; }
  BAR();
  if (blockIdx.x < 128) f_role(P, L, ws, blockIdx.x, w, lane);
  else a_role(P, L, ws, blockIdx.x - 128, w, lane);

}

extern "C" void kernel_launch(void* const* d_in, const int* in_sizes, int n_in, void* d_out, int out_size, void* d_ws,
                              size_t ws_size, hipStream_t stream) {
  (void)in_sizes; (void)n_in; (void)out_size; (void)ws_size;
  (void)hipFuncSetAttribute((const void*)fused_decode, hipFuncAttributeMaxDynamicSharedMemorySize, LDS_BYTES);
  fused_decode<<<256, 512, LDS_BYTES, stream>>>((const u16*)d_in[0], (const u16*)d_in[1], (const u16*)d_in[2], (const u16*)d_in[3],
                                                (const u16*)d_in[4], (const u16*)d_in[5], (const u16*)d_in[7], (const u16*)d_in[8],
                                                (const u16*)d_in[9], (const u16*)d_in[10], (const u16*)d_in[11], (u16*)d_out, (char*)d_ws);
}
```

```cpp
#include <hip/hip_runtime.h>
#include <hip/hip_bf16.h>
#include <stdint.h>

typedef unsigned short u16;
typedef unsigned int u32;
typedef unsigned long long u64;
typedef u32 v4u __attribute__((ext_vector_type(4)));
typedef u32 v2u __attribute__((ext_vector_type(2)));
typedef float f32x4 __attribute__((ext_vector_type(4)));
typedef float f32x2 __attribute__((ext_vector_type(2)));
typedef float f32x16 __attribute__((ext_vector_type(16)));
typedef short s16x4 __attribute__((ext_vector_type(4)));
typedef short s16x8 __attribute__((ext_vector_type(8)));
typedef __bf16 bf16x8 __attribute__((ext_vector_type(8)));
typedef __bf16 bf16x2 __attribute__((ext_vector_type(2)));
#define GAS __attribute__((address_space(1)))
#define LAS __attribute__((address_space(3)))
#define DI __device__ __forceinline__

#define A_DELAY 0
#define F_TAIL_DELAY 700

#define S6_SLEEP 1
#define ST_SLEEP 4
#define S8_SLEEP 1
#define Q_TIME 0
#define Q_SLEEP 1
#define S4_SLEEP 0
#define CODE_PF 20480
constexpr u32 SENT = 0xFFFFFFFFu;
constexpr u32 SPIN_CAP = 1u << 17;

constexpr int WS_EPOCH = 0;
constexpr int WS_FLAGF = 256;
constexpr int S1_COPY = 5 * 4 * 640 * 4, S3_COPY = 2 * 32 * 512 * 2, S6_COPY = 8 * 2 * 640 * 4;
constexpr int WS_S1 = WS_FLAGF + 128 * 128 + 3840;
constexpr int WS_S2 = WS_S1 + 8 * S1_COPY;
constexpr int WS_S3 = WS_S2 + 32 * 4 * 1024;
constexpr int WS_S5 = WS_S3 + 8 * S3_COPY;
constexpr int WS_S6 = WS_S5 + 32 * 4 * 1024;
constexpr int WS_MB = WS_S6 + 8 * S6_COPY;
constexpr int WS_S8 = WS_MB + 4 * 2 * 640 * 4;
constexpr int WS_ST = WS_S8 + 40 * 108 * 32 * 4;
constexpr int WS_S4 = WS_ST + 128 * 8 * 128;
constexpr int WS_END = WS_S4 + 32 * 4 * 2 * 64 * 256;
static_assert(WS_S1 % 256 == 0 && WS_S2 % 256 == 0 && WS_S3 % 256 == 0 && WS_S5 % 256 == 0 && WS_S6 % 256 == 0 && WS_MB % 256 == 0 &&
              WS_S8 % 256 == 0 && WS_ST % 256 == 0 && WS_S4 % 256 == 0, "align");

constexpr int LDS_BYTES = 163840;

typedef __amdgpu_buffer_rsrc_t rsrc_t;
DI v4u wld128(rsrc_t r, int off) { return __builtin_amdgcn_raw_buffer_load_b128(r, off, 0, 16); }
DI v2u wld64(rsrc_t r, int off) { return __builtin_amdgcn_raw_buffer_load_b64(r, off, 0, 16); }
DI u32 wld32(rsrc_t r, int off) { return __builtin_amdgcn_raw_buffer_load_b32(r, off, 0, 16); }
DI void wst128(rsrc_t r, int off, v4u v) { __builtin_amdgcn_raw_buffer_store_b128(v, r, off, 0, 16); }
DI void wst64(rsrc_t r, int off, v2u v) { __builtin_amdgcn_raw_buffer_store_b64(v, r, off, 0, 16); }
DI void wst32(rsrc_t r, int off, u32 v) { __builtin_amdgcn_raw_buffer_store_b32(v, r, off, 0, 16); }
DI u32 canon(u32 x) { return x < 0xFFFFFFFEu ? x : 0xFFFFFFFEu; }
DI v4u tag4(f32x4 v, u32 T) {
  v4u o;
  o.x = __builtin_amdgcn_perm(__float_as_uint(v[0]), T, 0x07060500u); o.y = __builtin_amdgcn_perm(__float_as_uint(v[1]), T, 0x07060501u);
  o.z = __builtin_amdgcn_perm(__float_as_uint(v[2]), T, 0x07060502u); o.w = __builtin_amdgcn_perm(__float_as_uint(v[3]), T, 0x07060503u);
  return o;
}
DI u32 tbad4(v4u v, u32 T) { return (__builtin_amdgcn_perm(v.y, v.x, 0x0c0c0400u) | __builtin_amdgcn_perm(v.w, v.z, 0x04000c0cu)) ^ T; }
constexpr int OOB = 0x40000000;
DI f32x4 asf4(v4u v) { const v4u m = v & 0xFFFFFF00u; return __builtin_bit_cast(f32x4, m); }
DI u32 mk_tag(u32 epoch_word) { return (epoch_word + 1u) * 0x01010101u; }
#define VM0() do { __builtin_amdgcn_s_waitcnt(0x0F70); asm volatile("" ::: "memory"); } while (0)
DI float bflo(u32 x) { return __uint_as_float(x << 16); }
DI float bfhi(u32 x) { return __uint_as_float(x & 0xffff0000u); }
DI u32 pk_bf16(float lo, float hi) { f32x2 f = {lo, hi}; bf16x2 h = __builtin_convertvector(f, bf16x2); return __builtin_bit_cast(u32, h); }
DI float silu_f(float x) { return x * __builtin_amdgcn_rcpf(1.f + __expf(-x)); }
DI v4u ldnt128(const void* p) { return __builtin_nontemporal_load((const GAS v4u*)p); }
DI f32x4 mfma16(v4u a, v4u b, f32x4 c) { return __builtin_amdgcn_mfma_f32_16x16x32_bf16(__builtin_bit_cast(bf16x8, a), __builtin_bit_cast(bf16x8, b), c, 0, 0, 0); }
DI f32x16 mfma32(v4u a, v4u b, f32x16 c) { return __builtin_amdgcn_mfma_f32_32x32x16_bf16(__builtin_bit_cast(bf16x8, a), __builtin_bit_cast(bf16x8, b), c, 0, 0, 0); }
#define BAR() asm volatile("s_waitcnt lgkmcnt(0)\n\ts_barrier" ::: "memory")
#define CBAR() asm volatile("" ::: "memory")
template <int N> DI void vmwait() { asm volatile("s_waitcnt vmcnt(%0)" :: "i"(N) : "memory"); }

template <int CTRL> DI float dpp(float x) { return __builtin_bit_cast(float, __builtin_amdgcn_mov_dpp(__builtin_bit_cast(int, x), CTRL, 0xf, 0xf, true)); }
DI float x16_sum(float x) { auto s = __builtin_amdgcn_permlane16_swap(__float_as_uint(x), __float_as_uint(x), false, false); return __uint_as_float(s[0]) + __uint_as_float(s[1]); }
DI float x32_sum(float x) { auto s = __builtin_amdgcn_permlane32_swap(__float_as_uint(x), __float_as_uint(x), false, false); return __uint_as_float(s[0]) + __uint_as_float(s[1]); }
DI float x16_max(float x) { auto s = __builtin_amdgcn_permlane16_swap(__float_as_uint(x), __float_as_uint(x), false, false); return fmaxf(__uint_as_float(s[0]), __uint_as_float(s[1])); }
DI float x32_max(float x) { auto s = __builtin_amdgcn_permlane32_swap(__float_as_uint(x), __float_as_uint(x), false, false); return fmaxf(__uint_as_float(s[0]), __uint_as_float(s[1])); }
DI float wave_sum(float x) { x += dpp<0xB1>(x); x += dpp<0x4E>(x); x += dpp<0x141>(x); x += dpp<0x140>(x); return x32_sum(x16_sum(x)); }

DI u64 uni64(const void* p) {
  const u64 a = (u64)p;
  return ((u64)(u32)__builtin_amdgcn_readfirstlane((int)(a >> 32)) << 32) | (u32)__builtin_amdgcn_readfirstlane((int)a);
}
DI void dma_s(u32 ldsaddr, const void* sbase, u32 voff) {
  u32 keep;
  asm volatile("s_mov_b32 %0, m0\n\ts_mov_b32 m0, %1\n\ts_nop 2\n\tglobal_load_lds_dwordx4 %2, %3 nt\n\ts_mov_b32 m0, %0"
               : "=&s"(keep) : "s"(__builtin_amdgcn_readfirstlane((int)ldsaddr)), "v"(voff), "s"(uni64(sbase)) : "memory");
}
DI void dma_s_keep(u32 ldsaddr, const void* sbase, u32 voff) {
  u32 keep;
  asm volatile("s_mov_b32 %0, m0\n\ts_mov_b32 m0, %1\n\ts_nop 2\n\tglobal_load_lds_dwordx4 %2, %3\n\ts_mov_b32 m0, %0"
               : "=&s"(keep) : "s"(__builtin_amdgcn_readfirstlane((int)ldsaddr)), "v"(voff), "s"(uni64(sbase)) : "memory");
}
DI void dma_v(u32 ldsaddr, const void* vaddr) {
  u32 keep;
  asm volatile("s_mov_b32 %0, m0\n\ts_mov_b32 m0, %1\n\ts_nop 0\n\tglobal_load_lds_dwordx4 %2, off nt\n\ts_mov_b32 m0, %0"
               : "=&s"(keep) : "s"(__builtin_amdgcn_readfirstlane((int)ldsaddr)), "v"(vaddr) : "memory");
}
DI u32 swz(u32 row) { return ((row & 3u) << 2) | ((row >> 2) & 3u); }
DI v4u ldsr128(const LAS char* p) { return *(const LAS v4u*)p; }
DI v4u tr2(const LAS char* p0, const LAS char* p1) {
  s16x4 a = __builtin_amdgcn_ds_read_tr16_b64_v4i16((LAS s16x4*)p0);
  s16x4 b = __builtin_amdgcn_ds_read_tr16_b64_v4i16((LAS s16x4*)p1);
  s16x8 v = __builtin_shufflevector(a, b, 0, 1, 2, 3, 4, 5, 6, 7);
  return __builtin_bit_cast(v4u, v);
}
DI void wait_ticks(u64 t0, u32 ticks) { if (ticks) { while (__builtin_amdgcn_s_memrealtime() - t0 < ticks) __builtin_amdgcn_s_sleep(2); } }

struct Ptrs { const u16 *x, *ckv, *wq, *wuk, *wuv, *wo, *wg, *wu, *wd, *c, *wmod; u16* out; char* ws; const char* code; u64 t0; };

DI v4u frag64(const LAS char* img, int lane, int c, int ks) {
  const int g = lane >> 4, q = (lane & 15) >> 2, p = lane & 3;
  const LAS char* a = img + (32 * ks + 8 * g + q) * 64 + 32 * c + 8 * p;
  return tr2(a, a + 4 * 64);
}
DI v4u frag256(const LAS char* img, int lane, int c, int ks) {
  const int g = lane >> 4, q = (lane & 15) >> 2, p = lane & 3;
  const u32 r0 = 8 * g + q, r1 = r0 + 4, ch = 2 * c + (p >> 1);
  return tr2(img + 8192 * ks + 256 * r0 + 16 * (ch ^ swz(r0)) + 8 * (p & 1), img + 8192 * ks + 256 * r1 + 16 * (ch ^ swz(r1)) + 8 * (p & 1));
}
template <int N> DI f32x4 gather_sum(rsrc_t ws, int off, int stride, u32 T) {
  f32x4 s;
  _Pragma("nounroll") for (u32 sp = 0; sp < SPIN_CAP; ++sp) {
    CBAR();
    v4u t[N];
#pragma unroll
    for (int k = 0; k < N; ++k) t[k] = wld128(ws, off + k * stride);
    u32 bad = 0;
#pragma unroll
    for (int k = 0; k < N; ++k) bad |= tbad4(t[k], T);
    s = asf4(t[0]);
#pragma unroll
    for (int k = 1; k < N; ++k) s += asf4(t[k]);
    if (__all(bad == 0)) break;
  }
  return s;
}
DI u32 lds_ld(const LAS char* p) { return *(const volatile LAS u32*)p; }
#define PIN(x) asm volatile("" : "+v"(x))
DI void lds_wait_ge(const LAS char* p, u32 n) { _Pragma("nounroll") for (u32 sp = 0; sp < (1u << 22); ++sp) { if ((u32)__builtin_amdgcn_readfirstlane(lds_ld(p)) >= n) break; } CBAR(); }
DI void lds_arrive(LAS char* p, int lane) { asm volatile("s_waitcnt lgkmcnt(0)" ::: "memory"); if (lane == 0) __hip_atomic_fetch_add((LAS u32*)p, 1u, __ATOMIC_RELAXED, __HIP_MEMORY_SCOPE_WORKGROUP); }
#define LGKM0() asm volatile("s_waitcnt lgkmcnt(0)" ::: "memory")

constexpr int L_CNT = LDS_BYTES - 64;
constexpr int FL_WQ = 0, FL_WUK = 40960, FL_WO = 0, FL_WUV = 81920, FL_SM = FL_WUV + 32768;
constexpr int FL_HB = FL_SM, FL_QB = FL_HB + 1024, FL_QA = FL_QB + 512, FL_CRED = FL_QA + 512, FL_CST = FL_CRED + 4096,
              FL_CB = FL_CST + 64, FL_CTX = FL_CB + 512, FL_WRED = FL_CTX + 4096, FL_FIN = FL_WRED + 4096, FL_PF = FL_FIN + 1024,
              FL_SB = FL_PF + 1024  ;
static_assert(FL_PF % 16 == 0 && FL_SB + 2560 <= L_CNT, "lds");

DI void f_role(const Ptrs& P, LAS char* L, rsrc_t ws, int p, int w, int lane) {
  const int i16 = lane & 15, g = lane >> 4;
  const int h = p & 31, sl = p >> 5, cp = p & 7;
  const f32x4 Z4 = {0.f, 0.f, 0.f, 0.f};
  float ln[4] = {0.f, 0.f, 0.f, 0.f};
  u32 T = 0, E1 = 0;
  if (w < 2) {
    T = wld32(ws, WS_EPOCH);
    {
      const v4u S = {SENT, SENT, SENT, SENT};
#pragma unroll
      for (int k = 0; k < 2; ++k) wst128(ws, WS_S3 + (4 * w + 2 * k + (lane >> 5)) * S3_COPY + (((lane >> 4) & 1) * 32 + h) * 1024 + sl * 256 + i16 * 16, S);
    }
    const v4u xa = *(const GAS v4u*)(P.x + w * 640 + 8 * lane);
    const v4u xb = *(const GAS v4u*)(P.x + w * 640 + 512 + 8 * (lane & 15));
    const v2u xs = *(const GAS v2u*)(P.x + w * 640 + 160 * sl + 4 * (lane < 40 ? lane : 0));
    VM0();
    E1 = T + 1u;
    T = __builtin_amdgcn_readfirstlane(mk_tag(T));
    { const v2u f = {T, ~T}; wst64(ws, lane == 0 ? WS_FLAGF + p * 128 + 8 * w : OOB, f); }
    if (w == 0 && lane == 0) *(LAS u32*)(L + L_CNT + 4) = T;
    float v[16];
#pragma unroll
    for (int k = 0; k < 4; ++k) { v[2 * k] = bflo(xa[k]); v[2 * k + 1] = bfhi(xa[k]); v[8 + 2 * k] = bflo(xb[k]); v[9 + 2 * k] = bfhi(xb[k]); }
    const bool ex = lane < 16;
    float s = 0.f;
#pragma unroll
    for (int k = 0; k < 8; ++k) s += v[k] + (ex ? v[8 + k] : 0.f);
    const float mean = wave_sum(s) * (1.f / 640.f);
    float s2 = 0.f;
#pragma unroll
    for (int k = 0; k < 8; ++k) { const float d = v[k] - mean, e = v[8 + k] - mean; s2 += d * d + (ex ? e * e : 0.f); }
    const float rstd = rsqrtf(wave_sum(s2) * (1.f / 640.f) + 1e-5f);
    ln[0] = (bflo(xs.x) - mean) * rstd; ln[1] = (bfhi(xs.x) - mean) * rstd; ln[2] = (bflo(xs.y) - mean) * rstd; ln[3] = (bfhi(xs.y) - mean) * rstd;
  } else if (w < 6) {
    const char* sb = (const char*)P.wq + (size_t)(160 * sl) * 8192 + h * 256;
    const u32 n0 = w - 2;
    const u32 vo = g * 8192 + ((i16 ^ ((g << 2) | n0)) << 4);
#pragma unroll 2
    for (int k = 0; k < 10; ++k) dma_s(FL_WQ + (n0 + 4 * k) * 1024, sb + (size_t)(n0 + 4 * k) * 4 * 8192, vo);
    vmwait<0>();
    lds_arrive(L + L_CNT, lane);
  } else {
    __builtin_amdgcn_s_sleep(2);
    const char* su = (const char*)P.wuk + (size_t)(128 * sl) * 8192 + h * 256;
    const u32 n0 = w - 6;
    const u32 vo0 = g * 8192 + ((i16 ^ ((g << 2) | n0)) << 4), vo1 = g * 8192 + ((i16 ^ ((g << 2) | (n0 + 2))) << 4);
#pragma unroll 1
    for (int k = 0; k < 8; ++k) {
      dma_s(FL_WUK + (n0 + 4 * k) * 1024, su + (size_t)(n0 + 4 * k) * 4 * 8192, vo0);
      dma_s(FL_WUK + (n0 + 4 * k + 2) * 1024, su + (size_t)(n0 + 4 * k + 2) * 4 * 8192, vo1);
    }
    if (w == 7) {
      const int o0 = (p >> 3) * 1024;
      dma_s_keep(FL_PF, P.code + (o0 < CODE_PF - 1024 ? o0 : CODE_PF - 1024), lane * 16);
      dma_s_keep(FL_PF, P.code + (o0 + 16384 < CODE_PF - 1024 ? o0 + 16384 : CODE_PF - 1024), lane * 16);
    }
  }
  u32 hb_a = FL_HB + (lane & 1) * 320 + g * 16, s2_o = i16 < 2 ? WS_S2 + (h * 4 + sl) * 1024 + w * 128 + i16 * 64 + g * 16 : OOB;
  PIN(hb_a); PIN(s2_o);
  lds_wait_ge(L + L_CNT, 4u);
  v4u aq[5];
#pragma unroll
  for (int k = 0; k < 5; ++k) aq[k] = frag256(L + FL_WQ, lane, w, k);
  if (w < 2) {
    f32x4 sa, ca;
    const int s1_o = lane < 40 ? WS_S1 + cp * S1_COPY + (w * 640 + 160 * sl + 4 * lane) * 4 : OOB;
    _Pragma("nounroll") for (u32 sp = 0; sp < SPIN_CAP; ++sp) {
      CBAR();
      v4u t[10];
#pragma unroll
      for (int k = 0; k < 10; ++k) t[k] = wld128(ws, s1_o + ((k >> 1) * 4 + (k & 1) * 2) * 2560);
      u32 bad = 0;
#pragma unroll
      for (int k = 0; k < 10; ++k) bad |= tbad4(t[k], T);
      sa = asf4(t[0]); ca = asf4(t[1]);
#pragma unroll
      for (int k = 1; k < 5; ++k) { sa += asf4(t[2 * k]); ca += asf4(t[2 * k + 1]); }
      if (__all(lane >= 40 || bad == 0)) break;
    }
    const float h0 = ln[0] * (1.f + ca[0]) + sa[0], h1 = ln[1] * (1.f + ca[1]) + sa[1];
    const float h2 = ln[2] * (1.f + ca[2]) + sa[2], h3 = ln[3] * (1.f + ca[3]) + sa[3];
    if (lane < 40) { const v2u o = {pk_bf16(h0, h1), pk_bf16(h2, h3)}; *(LAS v2u*)(L + FL_HB + w * 320 + lane * 8) = o; }
  }
  BAR();
  {
    v4u bh[5];
#pragma unroll
    for (int k = 0; k < 5; ++k) bh[k] = ldsr128(L + hb_a + 64 * k);
    T = __builtin_amdgcn_readfirstlane(lds_ld(L + L_CNT + 4));
    f32x4 acc = Z4;
#pragma unroll
    for (int k = 0; k < 5; ++k) acc = mfma16(aq[k], bh[k], acc);
    wst128(ws, s2_o, tag4(acc, T));
  }
  u32 qb_a = FL_QB + (lane & 1) * 256 + g * 16, uk_a[4], qa_w = FL_QA + i16 * 256 + (16 * w + 4 * g) * 2;
  {
    const u32 row = 16 * w + i16, sw = swz(row);
#pragma unroll
    for (int k = 0; k < 4; ++k) { uk_a[k] = FL_WUK + row * 256 + (((4 * k + g) ^ sw) << 4); PIN(uk_a[k]); }
  }
  PIN(qb_a); PIN(qa_w);
  if (w == 0) {
    const f32x4 q = gather_sum<4>(ws, WS_S2 + h * 4096 + lane * 16, 1024, T);
    const v2u o = {pk_bf16(q[0], q[1]), pk_bf16(q[2], q[3])};
    *(LAS v2u*)(L + FL_QB + ((lane >> 2) & 1) * 256 + (16 * (lane >> 3) + 4 * (lane & 3)) * 2) = o;
  } else if (w >= 6) {
    vmwait<0>();
  }
  BAR();
  {
    v4u bq[4], au[4];
#pragma unroll
    for (int k = 0; k < 4; ++k) { au[k] = ldsr128(L + uk_a[k]); bq[k] = ldsr128(L + qb_a + 64 * k); }
    f32x4 acc = Z4;
#pragma unroll
    for (int k = 0; k < 4; ++k) acc = mfma16(au[k], bq[k], acc);
    constexpr float SC = 0.08838834764831845f * 1.4426950408889634f;
    if (i16 < 2) { const v2u o = {canon(pk_bf16(acc[0] * SC, acc[1] * SC)), canon(pk_bf16(acc[2] * SC, acc[3] * SC))};
      *(LAS v2u*)(L + qa_w) = o; }
  }
  BAR();
  wst128(ws, lane < 32 ? WS_S3 + w * S3_COPY + ((lane >> 4) * 32 + h) * 1024 + sl * 256 + i16 * 16 : OOB, ldsr128(L + FL_QA + (lane & 31) * 16));
  const int kq = p / 10, ms = p % 10;
  if (w >= 4) {
    if (p < 88) wait_ticks(P.t0, F_TAIL_DELAY);
    const u32 n0 = w - 4;
    const char* sb = (const char*)P.wuv + (size_t)(128 * sl) * 8192 + h * 256;
    const u32 vo = g * 8192 + ((i16 ^ ((g << 2) | n0)) << 4);
#pragma unroll 2
    for (int k = 0; k < 8; ++k) dma_s(FL_WUV + (n0 + 4 * k) * 1024, sb + (size_t)(n0 + 4 * k) * 4 * 8192, vo);
    if (p < 80) {
      const char* so = (const char*)P.wo + (size_t)(512 * kq) * 1280 + ms * 128;
      const u32 vo2 = (lane >> 3) * 1280 + (((lane & 7) ^ ((((lane >> 4) & 1) << 1) | ((n0 & 1) << 2))) << 4);
#pragma unroll 2
      for (int n = n0; n < 64; n += 4) dma_s(FL_WO + n * 1024, so + (size_t)n * 8 * 1280, vo2);
    } else if (p >= 88) {
      const char* sm = (const char*)P.wmod + (size_t)(1280 + 64 * (p - 88)) * 2;
      const u32 vo2 = (lane >> 3) * 7680 + (((lane & 7) ^ ((((lane >> 4) & 1) << 1) | ((n0 & 1) << 2))) << 4);
#pragma unroll 2
      for (int n = n0; n < 80; n += 4) dma_s(FL_WO + n * 1024, sm + (size_t)n * 8 * 7680, vo2);
    }
    vmwait<0>();
  } else if (p >= 88 && w < 3) {
    const int tid = w * 64 + lane;
    const v4u cvv = *(const GAS v4u*)(P.c + (tid < 160 ? tid : 0) * 8);
    v4u sv;
#pragma unroll
    for (int k = 0; k < 4; ++k) sv[k] = pk_bf16(silu_f(bflo(cvv[k])), silu_f(bfhi(cvv[k])));
    if (tid < 160) *(LAS v4u*)(L + FL_SB + tid * 16) = sv;
  }
  BAR();
  if (p >= 88) {
    f32x4 a_[4] = {Z4, Z4, Z4, Z4};
    const int q = (lane & 15) >> 2, pp = lane & 3;
    for (int ks = w; ks < 20; ks += 8) {
      const v4u b = ldsr128(L + FL_SB + (lane & 1) * 1280 + (32 * ks + 8 * g) * 2);
      const LAS char* base = L + FL_WO + (32 * ks + 8 * g + q) * 128 + 8 * (pp & 1);
      const u32 f = (((q >> 1) & 1) << 1) | ((g & 1) << 2);
#pragma unroll
      for (int c = 0; c < 4; ++c) { const LAS char* a = base + (((2 * c + (pp >> 1)) ^ f) << 4); a_[c] = mfma16(tr2(a, a + 4 * 128), b, a_[c]); }
    }
    if (i16 < 2) {
      LAS f32x4* wr = (LAS f32x4*)(L + FL_WRED + w * 512 + i16 * 256 + g * 16);
#pragma unroll
      for (int c = 0; c < 4; ++c) wr[4 * c] = a_[c];
    }
    BAR();
    if (w == 0) {
      const LAS f32x4* wr = (const LAS f32x4*)(L + FL_WRED + (lane & 31) * 16);
      f32x4 v = wr[0];
#pragma unroll
      for (int k = 1; k < 8; ++k) v += wr[32 * k];
      const int col = 64 * (p - 88);
      wst128(ws, lane < 32 ? WS_MB + (((col / 640) * 2 + (lane >> 4)) * 640 + col % 640) * 4 + (lane & 15) * 16 : OOB, tag4(v, T));
    }
  }
  v4u uv[4], ao[8];
  {
    const int b = w >> 2, k0 = 16 * (w & 3);
    const int sto = WS_ST + ((b * 64 + k0 + i16) * 8 + (h & 7)) * 128 + (h >> 4) * 16;
    int s4_o = WS_S4 + ((((h * 4 + sl) * 2 + b) * 64 + k0) * 256) + lane * 16;
    u32 cb_a = FL_CB + (lane & 1) * 256 + g * 16, s5_o = i16 < 2 ? WS_S5 + (h * 4 + sl) * 1024 + w * 128 + i16 * 64 + g * 16 : OOB, cr_a = FL_CRED + w * 512 + (lane & 15) * 32;
    PIN(s4_o); PIN(cb_a); PIN(s5_o); PIN(cr_a);
    v4u s;
    _Pragma("nounroll") for (u32 sp = 0; sp < SPIN_CAP; ++sp) {
      CBAR();
      s = wld128(ws, lane < 16 ? sto : OOB);
      if (__all(lane >= 16 || tbad4(s, T) == 0)) break;
      __builtin_amdgcn_s_sleep(ST_SLEEP);
    }
    float wj[4];
    {
      const bool odd = (h >> 3) & 1;
      const float m = __uint_as_float((odd ? s.z : s.x) & 0xFFFFFF00u), l = __uint_as_float((odd ? s.w : s.y) & 0xFFFFFF00u);
      float Mw = fmaxf(m, dpp<0xB1>(m)); Mw = fmaxf(Mw, dpp<0x4E>(Mw)); Mw = fmaxf(Mw, dpp<0x141>(Mw)); Mw = fmaxf(Mw, dpp<0x140>(Mw));
      const float e = __builtin_amdgcn_exp2f(m - Mw);
      float Lw = e * l; Lw += dpp<0xB1>(Lw); Lw += dpp<0x4E>(Lw); Lw += dpp<0x141>(Lw); Lw += dpp<0x140>(Lw);
      if (lane == 0) *(LAS f32x2*)(L + FL_CST + w * 8) = f32x2{Mw, Lw};
      BAR();
      const LAS f32x4* cs = (const LAS f32x4*)(L + FL_CST + b * 32);
      const f32x4 c01 = cs[0], c23 = cs[1];
      const float M = fmaxf(fmaxf(c01[0], c01[2]), fmaxf(c23[0], c23[2]));
      const float den = (__builtin_amdgcn_exp2f(c01[0] - M) * c01[1] + __builtin_amdgcn_exp2f(c01[2] - M) * c01[3]) +
                        (__builtin_amdgcn_exp2f(c23[0] - M) * c23[1] + __builtin_amdgcn_exp2f(c23[2] - M) * c23[3]);
      const float wn = __builtin_amdgcn_exp2f(m - M) * __builtin_amdgcn_rcpf(den);
#pragma unroll
      for (int j = 0; j < 4; ++j) wj[j] = __shfl(wn, 4 * j + g);
    }
#pragma unroll
    for (int k = 0; k < 4; ++k) uv[k] = frag256(L + FL_WUV, lane, w, k);
    if (S4_SLEEP) __builtin_amdgcn_s_sleep(S4_SLEEP);
    v4u t[4];
    _Pragma("nounroll") for (u32 sp = 0; sp < SPIN_CAP; ++sp) {
      CBAR();
#pragma unroll
      for (int j = 0; j < 4; ++j) t[j] = wld128(ws, s4_o + j * 1024);
      u32 mx = 0;
#pragma unroll
      for (int j = 0; j < 4; ++j) mx = max(mx, max(max(t[j].x, t[j].y), max(t[j].z, t[j].w)));
      if (__all(mx != SENT)) break;
    }
    float acc[8];
#pragma unroll
    for (int k = 0; k < 4; ++k) { acc[2 * k] = wj[0] * bflo(t[0][k]); acc[2 * k + 1] = wj[0] * bfhi(t[0][k]); }
#pragma unroll
    for (int j = 1; j < 4; ++j) {
#pragma unroll
      for (int k = 0; k < 4; ++k) { acc[2 * k] += wj[j] * bflo(t[j][k]); acc[2 * k + 1] += wj[j] * bfhi(t[j][k]); }
    }
#pragma unroll
    for (int k = 0; k < 8; ++k) acc[k] = x32_sum(x16_sum(acc[k]));
    if (lane < 16) {
      LAS f32x4* cr = (LAS f32x4*)(L + cr_a);
      cr[0] = f32x4{acc[0], acc[1], acc[2], acc[3]}; cr[1] = f32x4{acc[4], acc[5], acc[6], acc[7]};
    }
    BAR();
    if (w == 0) {
      const int bb = lane >> 5;
      const LAS f32x4* cr = (const LAS f32x4*)(L + FL_CRED + bb * 2048 + (lane & 31) * 16);
      const f32x4 v = (cr[0] + cr[32]) + (cr[64] + cr[96]);
      const v2u o = {pk_bf16(v[0], v[1]), pk_bf16(v[2], v[3])};
      *(LAS v2u*)(L + FL_CB + lane * 8) = o;
    }
    BAR();
    v4u bc[4];
#pragma unroll
    for (int k = 0; k < 4; ++k) bc[k] = ldsr128(L + cb_a + 64 * k);
    f32x4 a2 = Z4;
#pragma unroll
    for (int k = 0; k < 4; ++k) a2 = mfma16(uv[k], bc[k], a2);
    wst128(ws, s5_o, tag4(a2, T));
  }
  if (p < 80) {
    u32 cx_w = FL_CTX + w * 256 + ((lane >> 2) & 1) * 128 + (16 * ((lane & 31) >> 3) + 4 * (lane & 3)) * 2, cx_r = FL_CTX + w * 256 + (lane & 1) * 128 + g * 16,
        wr_w = FL_WRED + w * 512 + (i16 & 1) * 256 + g * 16, wr_r = FL_WRED + (lane & 31) * 16,
        s6_o = lane < 32 ? WS_S6 + w * S6_COPY + ((kq * 2 + (lane >> 4)) * 640 + 64 * ms) * 4 + (lane & 15) * 16 : OOB;
    PIN(cx_w); PIN(cx_r); PIN(wr_w); PIN(wr_r); PIN(s6_o);
    {
      const u32 f = (((lane >> 3) & 1) << 1) | ((g & 1) << 2);
      const LAS char* base = L + FL_WO + (64 * w + 8 * g + ((lane & 15) >> 2)) * 128 + 8 * (lane & 1);
#pragma unroll
      for (int k = 0; k < 8; ++k) { const LAS char* a = base + (k >> 2) * 32 * 128 + (((2 * (k & 3) + ((lane >> 1) & 1)) ^ f) << 4); ao[k] = tr2(a, a + 4 * 128); }
    }
    f32x4 cx = gather_sum<2>(ws, WS_S5 + (4 * kq + (w >> 1)) * 4096 + (lane >> 5) * 1024 + (w & 1) * 512 + (lane & 31) * 16, 2048, T);
#pragma unroll
    for (int k = 0; k < 4; ++k) cx[k] = x32_sum(cx[k]);
    const v2u o = {pk_bf16(cx[0], cx[1]), pk_bf16(cx[2], cx[3])};
    if (lane < 32) *(LAS v2u*)(L + cx_w) = o;
    LGKM0();
    v4u bx[2];
#pragma unroll
    for (int k = 0; k < 2; ++k) bx[k] = ldsr128(L + cx_r + 64 * k);
    f32x4 a[4];
#pragma unroll
    for (int c = 0; c < 4; ++c) a[c] = mfma16(ao[4 + c], bx[1], mfma16(ao[c], bx[0], Z4));
    if (i16 < 2) {
      LAS f32x4* wr = (LAS f32x4*)(L + wr_w);
#pragma unroll
      for (int c = 0; c < 4; ++c) wr[4 * c] = a[c];
    }
    BAR();
    {
      const LAS f32x4* wr = (const LAS f32x4*)(L + wr_r);
      f32x4 v[8];
#pragma unroll
      for (int k = 0; k < 8; ++k) v[k] = wr[32 * k];
      const f32x4 r = ((v[0] + v[1]) + (v[2] + v[3])) + ((v[4] + v[5]) + (v[6] + v[7]));
      wst128(ws, s6_o, tag4(r, T));
    }
  } else if (p >= 88) {
    const int fs = p - 88, bm = lane & 7, bb = bm >> 2, m0 = 16 * fs + 4 * (bm & 3);
    f32x4 x1, gb;
    if (w == 0) {
      const v2u xv = *(const GAS v2u*)(P.x + bb * 640 + m0);
      _Pragma("nounroll") for (u32 sp = 0; sp < SPIN_CAP; ++sp) {
        CBAR();
        const v4u t0 = wld128(ws, WS_S6 + cp * S6_COPY + (((lane >> 3) * 2 + bb) * 640 + m0) * 4);
        const v4u t4 = wld128(ws, lane < 8 ? WS_MB + ((0 * 2 + bb) * 640 + m0) * 4 : OOB);
        const v4u t5 = wld128(ws, lane < 8 ? WS_MB + ((3 * 2 + bb) * 640 + m0) * 4 : OOB);
        const u32 bad = tbad4(t0, T) | (lane < 8 ? tbad4(t4, T) | tbad4(t5, T) : 0u);
        f32x4 at = asf4(t0);
#pragma unroll
        for (int k = 0; k < 4; ++k) { float v = at[k]; v += dpp<0x128>(v); at[k] = x32_sum(x16_sum(v)); }
        const f32x4 xf = {bflo(xv.x), bfhi(xv.x), bflo(xv.y), bfhi(xv.y)};
        x1 = xf + asf4(t4) * at;
        gb = asf4(t5);
        if (__all(bad == 0)) break;
        __builtin_amdgcn_s_sleep(4);
      }
    }
    if (p == 88 && w == 0) wst32(ws, lane == 0 ? WS_EPOCH : OOB, E1);
    BAR();
    f32x4 y;
    const int tid = w * 64 + lane;
    _Pragma("nounroll") for (u32 sp = 0; sp < SPIN_CAP; ++sp) {
      CBAR();
      const v4u t0 = wld128(ws, WS_S8 + fs * (108 * 128) + tid * 16);
      const v4u t1 = wld128(ws, WS_S8 + fs * (108 * 128) + (tid < 352 ? 512 + tid : tid) * 16);
      y = asf4(t0);
      if (tid < 352) y += asf4(t1);
      if (__all((tbad4(t0, T) | tbad4(t1, T)) == 0)) break;
      if (S8_SLEEP) __builtin_amdgcn_s_sleep(S8_SLEEP);
    }
#pragma unroll
    for (int k = 0; k < 4; ++k) { float v = y[k]; v += dpp<0x128>(v); y[k] = x32_sum(x16_sum(v)); }
    if (lane < 8) *(LAS f32x4*)(L + FL_FIN + w * 128 + lane * 16) = y;
    BAR();
    if (w == 0) {
      const LAS f32x4* fr = (const LAS f32x4*)(L + FL_FIN + bm * 16);
      f32x4 v[8];
#pragma unroll
      for (int k = 0; k < 8; ++k) v[k] = fr[8 * k];
      const f32x4 o = x1 + gb * (((v[0] + v[1]) + (v[2] + v[3])) + ((v[4] + v[5]) + (v[6] + v[7])));
      const v2u ob = {pk_bf16(o[0], o[1]), pk_bf16(o[2], o[3])};
      if (lane < 8) *(GAS v2u*)(P.out + bb * 640 + m0) = ob;
    }
  }
}

constexpr int AL_CKV = 0, AL_Q = 131072;
constexpr int AL_P = AL_Q, AL_MAX = AL_Q + 8192, AL_SUM = AL_MAX + 512;
constexpr int AL_O = 0, AL_GU = 32768, AL_DN = AL_Q + 10240  , AL_SM = AL_GU + 40960;
constexpr int AL_H2 = AL_SM, AL_STAT = AL_H2 + 2560, AL_RED = AL_STAT + 64, AL_ACT = AL_RED + 1024;
constexpr int AL_CNT = AL_Q + 4096;
static_assert(AL_ACT + 8 * 128 <= AL_Q && AL_SUM + 512 <= AL_DN && AL_DN + 20480 <= LDS_BYTES, "lds");

DI void a_role(const Ptrs& P, LAS char* L, rsrc_t ws, int j, int w, int lane) {
  const int i16 = lane & 15, g = lane >> 4, l32 = lane & 31, hh = lane >> 5;
  const int b = j >> 6, kc = j & 63, cp = j & 7;
  const f32x4 Z4 = {0.f, 0.f, 0.f, 0.f};
  f32x16 S;
  const int fl = (j & 7) + 8 * (j >> 5), fsub = (j >> 3) & 3, fj = 4 * fl + fsub;
  const bool do_ffn = fl < 27;
  v4u dummy = {0u, 0u, 0u, 0u};
  u32 T = 0;
  u32 ca[8], qa[8];
  T = wld32(ws, WS_EPOCH);
  {
    const int un = (j & 7) + 8 * (j >> 4), cg = 2 * (un / 5) + ((j >> 3) & 1), ks1 = un % 5;
    if (un < 50) {
      const int base = 128 * ks1 + 16 * w;
      const int bb = (lane >> 3) & 1, r1 = i16 >> 3, mat = (i16 >> 2) & 1, ch = i16 & 3, jj = lane & 7;
      u32 cv[2]; v4u wm[2];
#pragma unroll
      for (int t = 0; t < 2; ++t) cv[t] = *(const GAS u32*)(P.c + bb * 640 + base + 8 * t + 2 * g);
#pragma unroll
      for (int t = 0; t < 2; ++t) wm[t] = ldnt128(P.wmod + (size_t)(base + 8 * t + 2 * g + r1) * 3840 + mat * 640 + 32 * cg + 8 * ch);
      u32 msk[4];
#pragma unroll
      for (int r = 0; r < 4; ++r) msk[r] = (r == (jj >> 1)) ? (0xFFFFu << (16 * (jj & 1))) : 0u;
      int s1_o = WS_S1 + w * S1_COPY + ((ks1 * 4 + (l32 >> 3)) * 640 + 32 * cg) * 4 + (lane & 7) * 16;
      if (lane >= 32) s1_o = OOB;
      PIN(s1_o);
      f32x4 a0 = Z4, a1 = Z4;
#pragma unroll
      for (int t = 0; t < 2; ++t) {
        const u32 sp = pk_bf16(silu_f(bflo(cv[t])), silu_f(bfhi(cv[t])));
        const u32 d0 = (sp & 0xFFFFu) * 0x10001u, d1 = (sp >> 16) * 0x10001u;
        const v4u B0 = {d0 & msk[0], d0 & msk[1], d0 & msk[2], d0 & msk[3]};
        const v4u B1 = {d1 & msk[0], d1 & msk[1], d1 & msk[2], d1 & msk[3]};
        a0 = mfma16(wm[t], B0, a0);
        a1 = mfma16(wm[t], B1, a1);
      }
      LAS float* red = (LAS float*)(L + AL_Q) + w * 128;
      const int mt = (lane >> 4) & 1;
#pragma unroll
      for (int r = 0; r < 4; ++r) {
        const float v = x32_sum(lane < 32 ? a0[r] : a1[r]);
        if (lane < 32) red[(mt * 2 + bb) * 32 + 8 * r + jj] = v;
      }
      T = __builtin_amdgcn_readfirstlane(mk_tag(T));
      BAR();
      {
        const LAS f32x4* rd = (const LAS f32x4*)(L + AL_Q) + l32;
        const f32x4 s = ((rd[0] + rd[32]) + (rd[64] + rd[96])) + ((rd[128] + rd[160]) + (rd[192] + rd[224]));
        wst128(ws, s1_o, tag4(s, T));
      }
      BAR();
    } else {
      T = __builtin_amdgcn_readfirstlane(mk_tag(T));
    }
  }
  if (w >= 4) {
    const int lw = w - 4;
    const char* sb = (const char*)P.ckv + ((size_t)(b * 8192 + kc * 128 + 32 * lw)) * 1024;
    wait_ticks(P.t0, A_DELAY);
#pragma unroll 1
    for (int i4 = 0; i4 < 2; ++i4) {
#pragma unroll
      for (int i = 0; i < 16; ++i) {
        const u32 sw = ((i & 3) << 2) | ((i >> 2) & 3);
        dma_s(AL_CKV + (32 * lw + 16 * i4 + i) * 1024, sb + (16 * i4 + i) * 1024, g * 256 + ((i16 ^ sw) << 4));
      }
    }
    vmwait<0>();
  } else {
    {
      const u32 row = 32 * w + l32, sw = swz(row);
#pragma unroll
      for (int s = 0; s < 8; ++s) { ca[s] = AL_CKV + row * 1024 + (((2 * s + hh) ^ sw) << 4); qa[s] = AL_Q + l32 * 1024 + (((2 * s + hh) ^ (l32 & 15)) << 4); PIN(ca[s]); PIN(qa[s]); }
    }
    _Pragma("nounroll") for (u32 sp = 0; sp < SPIN_CAP; ++sp) {
      CBAR();
      const v4u f = wld128(ws, lane < 32 ? WS_FLAGF + (8 * w + (lane & 7) + 32 * (lane >> 3)) * 128 : OOB);
      if (__all(lane >= 32 || ((f.x ^ T) | (f.y ^ ~T) | (f.z ^ T) | (f.w ^ ~T)) == 0)) break;
      __builtin_amdgcn_s_sleep(8);
    }
    const int s3_o = WS_S3 + cp * S3_COPY + (b * 32 + 8 * w) * 1024;
    _Pragma("nounroll") for (u32 sp = 0; sp < SPIN_CAP; ++sp) {
      CBAR();
      const u32 f = wld32(ws, lane == 0 ? s3_o : OOB);
      if (__builtin_amdgcn_readfirstlane(f) != SENT) break;
      __builtin_amdgcn_s_sleep(Q_SLEEP);
    }
    _Pragma("nounroll") for (u32 sp = 0; sp < SPIN_CAP; ++sp) {
      CBAR();
      v4u t[8];
#pragma unroll
      for (int k = 0; k < 8; ++k) t[k] = wld128(ws, s3_o + k * 1024 + lane * 16);
      u32 mx = 0;
#pragma unroll
      for (int k = 0; k < 8; ++k) { const u32 a = max(max(t[k].x, t[k].y), max(t[k].z, t[k].w)); mx = max(mx, a); }
      if (__all(mx != SENT)) {
#pragma unroll
        for (int k = 0; k < 8; ++k) { const int hd = 8 * w + k; *(LAS v4u*)(L + AL_Q + hd * 1024 + ((lane ^ (hd & 15)) << 4)) = t[k]; }
        break;
      }
      __builtin_amdgcn_s_sleep(2);
    }
  }
  BAR();
  if (w < 4) {
#pragma unroll
    for (int k = 0; k < 16; ++k) S[k] = 0.f;
    {
      v4u a[32], q[32];
#pragma unroll
      for (int s = 0; s < 6; ++s) { a[s] = ldsr128(L + ca[s & 7] + (s >> 3) * 256); q[s] = ldsr128(L + qa[s & 7] + (s >> 3) * 256); }
      __builtin_amdgcn_sched_barrier(0);
#pragma unroll
      for (int s = 0; s < 32; ++s) {
        if (s + 6 < 32) { const int n = s + 6; a[n] = ldsr128(L + ca[n & 7] + (n >> 3) * 256); q[n] = ldsr128(L + qa[n & 7] + (n >> 3) * 256); }
        S = mfma32(a[s], q[s], S);
        __builtin_amdgcn_sched_barrier(0);
      }
    }
    float m0 = fmaxf(fmaxf(S[0], S[1]), fmaxf(S[2], S[3])), m1 = fmaxf(fmaxf(S[4], S[5]), fmaxf(S[6], S[7]));
    float m2 = fmaxf(fmaxf(S[8], S[9]), fmaxf(S[10], S[11])), m3 = fmaxf(fmaxf(S[12], S[13]), fmaxf(S[14], S[15]));
    const float m = x32_max(fmaxf(fmaxf(m0, m1), fmaxf(m2, m3)));
    if (lane < 32) *(LAS float*)(L + AL_MAX + (w * 32 + lane) * 4) = m;
  } else {
    const v4u Sv = {SENT, SENT, SENT, SENT};
#pragma unroll
    for (int k = 0; k < 8; ++k) wst128(ws, WS_S4 + (((((8 * (w - 4) + k) * 4 + g) * 2 + b) * 64 + kc) * 256) + i16 * 16, Sv);
  }
  u32 pa[8], va[4], oa[8];
  {
    const int blk = (lane >> 4) & 1, q = (lane & 15) >> 2, pp = lane & 3;
    const u32 sw = swz(l32);
    const u32 c0 = 8 * (w & 1) + 2 * blk + (pp >> 1);
    const u32 r0 = 8 * hh + q, r1 = r0 + 4, s0 = swz(r0), s1 = swz(r1);
    const u32 tb = AL_CKV + (w >> 1) * 256 + 8 * (pp & 1);
    va[0] = tb + 1024 * r0 + 16 * (c0 ^ s0); va[1] = tb + 1024 * r1 + 16 * (c0 ^ s1);
    va[2] = tb + 1024 * r0 + 16 * ((c0 + 4) ^ s0); va[3] = tb + 1024 * r1 + 16 * ((c0 + 4) ^ s1);
#pragma unroll
    for (int k = 0; k < 8; ++k) { pa[k] = AL_P + l32 * 256 + (((2 * k + hh) ^ sw) << 4); oa[k] = AL_O + l32 * 1024 + (((8 * w + k) ^ (l32 & 15)) << 4) + 8 * hh; PIN(pa[k]); PIN(oa[k]); }
#pragma unroll
    for (int k = 0; k < 4; ++k) PIN(va[k]);
  }
  BAR();
  float M;
  {
    const LAS float* mx = (const LAS float*)(L + AL_MAX) + l32;
    M = fmaxf(fmaxf(mx[0], mx[32]), fmaxf(mx[64], mx[96]));
    M = __uint_as_float(__float_as_uint(M) & 0xFFFFFF00u);
  }
  if (w < 4) {
    float sum = 0.f;
#pragma unroll
    for (int k = 0; k < 16; ++k) { S[k] = __builtin_amdgcn_exp2f(S[k] - M); sum += S[k]; }
    const u32 sw = swz(l32);
#pragma unroll
    for (int qd = 0; qd < 4; ++qd) {
      const v2u o = {pk_bf16(S[4 * qd], S[4 * qd + 1]), pk_bf16(S[4 * qd + 2], S[4 * qd + 3])};
      *(LAS v2u*)(L + AL_P + l32 * 256 + (((4 * w + qd) ^ sw) << 4) + 8 * hh) = o;
    }
    sum = x32_sum(sum);
    if (lane < 32) *(LAS float*)(L + AL_SUM + (w * 32 + lane) * 4) = sum;
  }
  if (w >= 4) VM0();
  BAR();
  f32x16 O0, O1;
#pragma unroll
  for (int k = 0; k < 16; ++k) { O0[k] = 0.f; O1[k] = 0.f; }
  {
    v4u pb[8], a0[8], a1[8];
#pragma unroll
    for (int ks = 0; ks < 8; ++ks) {
      pb[ks] = ldsr128(L + pa[ks]);
      a0[ks] = tr2(L + va[0] + ks * 16384, L + va[1] + ks * 16384);
      a1[ks] = tr2(L + va[2] + ks * 16384, L + va[3] + ks * 16384);
    }
    if (w == 7) {
      const LAS float* sm = (const LAS float*)(L + AL_SUM) + l32;
      const float Ls = (sm[0] + sm[32]) + (sm[64] + sm[96]);
      const float M2 = dpp<0x128>(M), L2 = dpp<0x128>(Ls);
      wst128(ws, (lane < 32 && !(lane & 8)) ? WS_ST + ((b * 64 + kc) * 8 + (lane & 7)) * 128 + (lane >> 4) * 16 : OOB, tag4(f32x4{M, Ls, M2, L2}, T));
    }
    __builtin_amdgcn_sched_barrier(0);
#pragma unroll
    for (int ks = 0; ks < 8; ++ks) { O0 = mfma32(a0[ks], pb[ks], O0); O1 = mfma32(a1[ks], pb[ks], O1); }
  }
  int s4_o = WS_S4 + ((((4 * w * 4 + g) * 2 + b) * 64 + kc) * 256) + i16 * 16;
  u32 or_a[4];
#pragma unroll
  for (int k = 0; k < 4; ++k) { const int hd = 4 * w + k; or_a[k] = AL_O + hd * 1024 + ((lane ^ (hd & 15)) << 4); PIN(or_a[k]); }
  PIN(s4_o);
  BAR();
#pragma unroll
  for (int qd = 0; qd < 4; ++qd) {
    const v2u o0 = {canon(pk_bf16(O0[4 * qd], O0[4 * qd + 1])), canon(pk_bf16(O0[4 * qd + 2], O0[4 * qd + 3]))};
    const v2u o1 = {canon(pk_bf16(O1[4 * qd], O1[4 * qd + 1])), canon(pk_bf16(O1[4 * qd + 2], O1[4 * qd + 3]))};
    *(LAS v2u*)(L + oa[qd]) = o0;
    *(LAS v2u*)(L + oa[4 + qd]) = o1;
  }
  BAR();
  {
    v4u v[4];
#pragma unroll
    for (int k = 0; k < 4; ++k) v[k] = ldsr128(L + or_a[k]);
#pragma unroll
    for (int k = 0; k < 4; ++k) wst128(ws, s4_o + k * (4 * 2 * 64 * 256), v[k]);
  }
  if (!do_ffn) return;
  const int bb = w >> 2, m0 = 160 * (w & 3) + 4 * (lane < 40 ? lane : 0);
  VM0();
  BAR();
  {
#pragma unroll 1
    for (int n = w; n < 40; n += 8) {
      const int line = n * 8 + (lane >> 3), mat = line >= 160, row = 160 * fsub + line - 160 * mat;
      const char* pp = (const char*)(mat ? P.wu : P.wg) + (size_t)row * 3456 + fl * 128 + (lane & 7) * 16;
      asm volatile("global_load_dwordx4 %0, %1, off" : "+v"(dummy) : "v"(pp) : "memory");
    }
    const char* sd = (const char*)P.wd + (size_t)(16 * fj) * 1280;
    for (int n = w; n < 20; n += 8) dma_s(AL_DN + n * 1024, sd + n * 1024, lane * 16);
    vmwait<0>();
    asm volatile("" :: "v"(dummy));
    const char* gp = (const char*)(((lane & 2) ? P.wu : P.wg)) + (size_t)(lane >> 2) * 3456 + fj * 32 + (lane & 1) * 16;
    for (int n = w; n < 40; n += 8) dma_v(AL_GU + n * 1024, gp + (size_t)n * 16 * 3456);
    vmwait<0>();
  }
  f32x4 xf, gav, sbv, cb1;
  {
    const v2u xv = *(const GAS v2u*)(P.x + bb * 640 + m0);
    v4u mb[3];
    _Pragma("nounroll") for (u32 sp = 0; sp < SPIN_CAP; ++sp) {
      CBAR();
#pragma unroll
      for (int k = 0; k < 3; ++k) mb[k] = wld128(ws, lane < 40 ? WS_MB + ((k * 2 + bb) * 640 + m0) * 4 : OOB);
      if (__all(lane >= 40 || (tbad4(mb[0], T) | tbad4(mb[1], T) | tbad4(mb[2], T)) == 0)) break;
      __builtin_amdgcn_s_sleep(8);
    }
    xf = f32x4{bflo(xv.x), bfhi(xv.x), bflo(xv.y), bfhi(xv.y)};
    gav = asf4(mb[0]); sbv = asf4(mb[1]); cb1 = asf4(mb[2]) + 1.f;
  }
  BAR();
  v4u ag[5], ad[5];
  {
#pragma unroll
    for (int i = 0; i < 5; ++i) ag[i] = frag64(L + AL_GU, lane, w & 1, (w >> 1) + 4 * i);
    const int q = (lane & 15) >> 2, pp = lane & 3;
    const int r0 = (8 * g + q) & 15;
#pragma unroll
    for (int k = 0; k < 5; ++k) { const LAS char* a = L + AL_DN + r0 * 1280 + (5 * w + k) * 32 + 8 * pp; ad[k] = tr2(a, a + 4 * 1280); }
  }
  {
    u32 h2_w = AL_H2 + (bb * 640 + m0) * 2, h2_r = AL_H2 + (lane & 1) * 1280 + (32 * (w >> 1) + 8 * g) * 2, red_w = AL_RED + w * 128 + (i16 & 1) * 64 + g * 16,
        red_r = AL_RED + (((lane >> 4) & 1) * 16 + (lane & 15)) * 4, act_w = AL_ACT + w * 128 + (((lane >> 4) & 1) * 32 + (lane >> 5) * 16 + (lane & 15)) * 2,
        act_r = AL_ACT + w * 128 + (lane & 1) * 64 + g * 16, s8_o = i16 < 2 ? WS_S8 + 5 * w * (108 * 128) + fj * 128 + i16 * 64 + g * 16 : OOB;
    int s6_o = lane < 40 ? WS_S6 + cp * S6_COPY + (bb * 640 + m0) * 4 : OOB;
    PIN(h2_w); PIN(h2_r); PIN(red_w); PIN(red_r); PIN(act_w); PIN(act_r); PIN(s8_o); PIN(s6_o);
    f32x4 at;
    _Pragma("nounroll") for (u32 sp = 0; sp < SPIN_CAP; ++sp) {
      CBAR();
      v4u t[8];
#pragma unroll
      for (int k = 0; k < 8; ++k) t[k] = wld128(ws, s6_o + k * 5120);
      at = ((asf4(t[0]) + asf4(t[1])) + (asf4(t[2]) + asf4(t[3]))) + ((asf4(t[4]) + asf4(t[5])) + (asf4(t[6]) + asf4(t[7])));
      u32 bad = 0;
#pragma unroll
      for (int k = 0; k < 8; ++k) bad |= tbad4(t[k], T);
      if (__all(lane >= 40 || bad == 0)) break;
      if (S6_SLEEP) __builtin_amdgcn_s_sleep(S6_SLEEP);
    }
    const f32x4 x1 = xf + gav * at;
    const bool act = lane < 40;
    const float pv = __builtin_bit_cast(float, __builtin_amdgcn_readfirstlane(__builtin_bit_cast(int, x1[0])));
    const float d0 = x1[0] - pv, d1 = x1[1] - pv, d2 = x1[2] - pv, d3 = x1[3] - pv;
    const float s1 = wave_sum(act ? (d0 + d1) + (d2 + d3) : 0.f);
    const float s2 = wave_sum(act ? (d0 * d0 + d1 * d1) + (d2 * d2 + d3 * d3) : 0.f);
    const float mw = pv + s1 * (1.f / 160.f), m2 = s2 - s1 * s1 * (1.f / 160.f);
    if (lane == 0) { LAS f32x2* sp = (LAS f32x2*)(L + AL_STAT); sp[w] = f32x2{mw, m2}; }
    BAR();
    const LAS f32x4* sp = (const LAS f32x4*)(L + AL_STAT + bb * 32);
    const f32x4 s01 = sp[0], s23 = sp[1];
    const float mean = ((s01[0] + s01[2]) + (s23[0] + s23[2])) * 0.25f;
    const float e0 = s01[0] - mean, e1 = s01[2] - mean, e2 = s23[0] - mean, e3 = s23[2] - mean;
    const float var = (((s01[1] + s01[3]) + (s23[1] + s23[3])) + 160.f * ((e0 * e0 + e1 * e1) + (e2 * e2 + e3 * e3))) * (1.f / 640.f);
    const float rstd = rsqrtf(var + 1e-5f);
    float hv[4];
#pragma unroll
    for (int k = 0; k < 4; ++k) hv[k] = (x1[k] - mean) * rstd * cb1[k] + sbv[k];
    if (act) { const v2u o = {pk_bf16(hv[0], hv[1]), pk_bf16(hv[2], hv[3])}; *(LAS v2u*)(L + h2_w) = o; }
    BAR();
    v4u bh[5];
#pragma unroll
    for (int i = 0; i < 5; ++i) bh[i] = ldsr128(L + h2_r + 256 * i);
    f32x4 a = Z4;
#pragma unroll
    for (int i = 0; i < 5; ++i) a = mfma16(ag[i], bh[i], a);
    if (i16 < 2) *(LAS f32x4*)(L + red_w) = a;
    BAR();
    const LAS float* wr = (const LAS float*)(L + red_r);
    const float gs = (wr[0] + wr[64]) + (wr[128] + wr[192]), us = (wr[32] + wr[96]) + (wr[160] + wr[224]);
    const float av = silu_f(gs) * us;
    *(LAS u16*)(L + act_w) = lane < 32 ? (u16)(pk_bf16(av, 0.f) & 0xFFFFu) : (u16)0;
    LGKM0();
    const v4u bq = ldsr128(L + act_r);
    f32x4 acc[5];
#pragma unroll
    for (int k = 0; k < 5; ++k) acc[k] = mfma16(ad[k], bq, Z4);
#pragma unroll
    for (int k = 0; k < 5; ++k) wst128(ws, s8_o + k * (108 * 128), tag4(acc[k], T));
  }
}

__global__ void __launch_bounds__(512, 2)
fused_decode(const u16* x, const u16* ckv, const u16* wq, const u16* wuk, const u16* wuv, const u16* wo, const u16* wg,
             const u16* wu, const u16* wd, const u16* c, const u16* wmod, u16* out, char* wsp) {
  extern __shared__ __attribute__((aligned(1024))) char lds[];
  LAS char* L = (LAS char*)lds;
  const int lane = threadIdx.x & 63, w = __builtin_amdgcn_readfirstlane(threadIdx.x >> 6);
  const rsrc_t ws = __builtin_amdgcn_make_buffer_rsrc((void*)wsp, 0, WS_END, 0x00020000);
  u64 pc;
  asm volatile("s_getpc_b64 %0" : "=s"(pc));
  Ptrs P{x, ckv, wq, wuk, wuv, wo, wg, wu, wd, c, wmod, out, wsp, (const char*)(pc & ~255ull), __builtin_amdgcn_s_memrealtime()};
  if (threadIdx.x == 0) { *(LAS u32*)(L + L_CNT) = 0u; *(LAS u32*)(L + AL_CNT) = 0u; }
  BAR();
  if (blockIdx.x < 128) f_role(P, L, ws, blockIdx.x, w, lane);
  else a_role(P, L, ws, blockIdx.x - 128, w, lane);

}

extern "C" void kernel_launch(void* const* d_in, const int* in_sizes, int n_in, void* d_out, int out_size, void* d_ws,
                              size_t ws_size, hipStream_t stream) {
  (void)in_sizes; (void)n_in; (void)out_size; (void)ws_size;
  (void)hipFuncSetAttribute((const void*)fused_decode, hipFuncAttributeMaxDynamicSharedMemorySize, LDS_BYTES);
  fused_decode<<<256, 512, LDS_BYTES, stream>>>((const u16*)d_in[0], (const u16*)d_in[1], (const u16*)d_in[2], (const u16*)d_in[3],
                                                (const u16*)d_in[4], (const u16*)d_in[5], (const u16*)d_in[7], (const u16*)d_in[8],
                                                (const u16*)d_in[9], (const u16*)d_in[10], (const u16*)d_in[11], (u16*)d_out, (char*)d_ws);
}
```
